```python
import torch

def _softmax(x, axis=-1):
    return torch.softmax(x, dim=axis)
def _gelu(x):
    return torch.nn.functional.gelu(x, approximate="tanh")
def _silu(x):
    return torch.nn.functional.silu(x)
def _sigmoid(x):
    return torch.sigmoid(x)
def _layernorm(x, axis=-1, eps=1e-5):
    m = x.mean(dim=axis, keepdim=True)
    v = x.var(dim=axis, keepdim=True, unbiased=False)
    return (x - m) / torch.sqrt(v + eps)
def _scatter_add(tgt, idx, src, axis=0):
    return tgt.clone().index_add_(axis, idx, src)
def _segment_sum(src, seg, n):
    out = torch.zeros((n,) + src.shape[1:], dtype=src.dtype)
    return out.index_add_(0, seg, src)
def _pad(x, pw):
    flat = tuple(v for pair in reversed(pw) for v in pair)
    return torch.nn.functional.pad(x, flat)

kernel_name = 'sequential_preLN__ssm_conv_h3__glu_gated_reglu__prompt'

def setup_inputs():

    _w = (lambda a: (lambda t: t.to(torch.bfloat16) if t.is_floating_point() else t)(torch.as_tensor(a)))
    def _A_setup():
        B, S, D = 1, 2048, 640
        ks = 4
        return {
            "q": _w((lambda *s: torch.randn(*s, dtype=torch.bfloat16) * 0.05)(B, S, D)),
            "k": _w((lambda *s: torch.randn(*s, dtype=torch.bfloat16) * 0.2)(B, S, D)),
            "v": _w((lambda *s: torch.randn(*s, dtype=torch.bfloat16) * 0.2)(B, S, D)),
            "shift_k": _w((lambda *s: torch.randn(*s, dtype=torch.bfloat16) * 0.2)(D, ks) * 0.5),
            "ssm_k": _w((lambda *s: torch.randn(*s, dtype=torch.bfloat16) * 0.4)(D, S) * (1.0 / S ** 0.5)),
            "ssm_D": _w((lambda *s: torch.randn(*s, dtype=torch.bfloat16) * 0.2)(D)),
        }
    _A_in = _A_setup()

    def _B_setup():
        B, S, D, F = 1, 2048, 640, 1536
        return {
            "x": _w((lambda *s: torch.randn(*s, dtype=torch.bfloat16) * 0.05)(B, S, D)),
            "W_gate": _w((lambda *s: torch.randn(*s, dtype=torch.bfloat16) * 0.2)(D, F) * (2.0 / D) ** 0.5),
            "W_up": _w((lambda *s: torch.randn(*s, dtype=torch.bfloat16) * 0.2)(D, F) * (2.0 / D) ** 0.5),
            "W_down": _w((lambda *s: torch.randn(*s, dtype=torch.bfloat16) * 0.2)(F, D) * (1.0 / F) ** 0.5),
        }
    _B_in = _B_setup()

    return {
        "A_q": _A_in["q"],
        "A_k": _A_in["k"],
        "A_v": _A_in["v"],
        "A_shift_k": _A_in["shift_k"],
        "A_ssm_k": _A_in["ssm_k"],
        "A_ssm_D": _A_in["ssm_D"],
        "B_x": _B_in["x"],
        "B_W_gate": _B_in["W_gate"],
        "B_W_up": _B_in["W_up"],
        "B_W_down": _B_in["W_down"],
    }

def reference(A_q, A_k, A_v, A_shift_k, A_ssm_k, A_ssm_D, B_x, B_W_gate, B_W_up, B_W_down):
    def _A_ref(q, k, v, shift_k, ssm_k, ssm_D):
        B, S, D = q.shape
        ks = shift_k.shape[-1]
        k_pad = _pad(k, ((0, 0), (ks - 1, 0), (0, 0)))
        k_shift = torch.zeros_like(k)
        for t in range(ks):
            k_shift = k_shift + k_pad[:, t:t + S, :] * shift_k[:, ks - 1 - t][None, None, :]
        kv = k_shift * v
        L = 2 * S
        kv_pad = _pad(kv, ((0, 0), (0, S), (0, 0)))
        kf_pad = _pad(ssm_k, ((0, 0), (0, S)))
        kv_f = (lambda x, axes=(-2,-1): torch.fft.rfft2(x.float(), dim=axes))(kv_pad[:, None, :, :], axes=(1, 2))
        kf_f = (lambda x, axes=(-2,-1): torch.fft.rfft2(x.float(), dim=axes))(kf_pad[:, None, :], axes=(1, 2))
        kf_f = (lambda x, *d: x.permute(*d))(kf_f, 1, 2, 0)
        y_f = kv_f * kf_f[None, :, :, :]
        y = (lambda x, s, axes=(-2,-1): torch.fft.irfft2(x, s=s, dim=axes).to(torch.bfloat16))(y_f, (1, L), axes=(1, 2))[:, 0, :S, :]
        y = y + kv * ssm_D[None, None, :]
        return q * y

    def _B_ref(x, W_gate, W_up, W_down):
        gate = torch.einsum("bsd,df->bsf", x, W_gate)
        up = torch.einsum("bsd,df->bsf", x, W_up)
        gate = gate * (lambda x, dt: x.to(dt))(gate > 0, gate.dtype)
        h = gate * up
        return torch.einsum("bsf,fd->bsd", h, W_down)

    x = A_q
    h = x + _A_ref(_layernorm(x, axis=-1), A_k, A_v, A_shift_k, A_ssm_k, A_ssm_D)
    h2 = h + _B_ref(_layernorm(h, axis=-1), B_W_gate, B_W_up, B_W_down)
    return h2
```

```cpp
#include <hip/hip_runtime.h>
#include <stdint.h>

typedef unsigned short u16;
typedef short s16x4 __attribute__((ext_vector_type(4)));
typedef short s16x8 __attribute__((ext_vector_type(8)));
typedef __bf16 bf16x8 __attribute__((ext_vector_type(8)));
typedef __bf16 bf16x2 __attribute__((ext_vector_type(2)));
typedef float f32x2 __attribute__((ext_vector_type(2)));
typedef float f32x4 __attribute__((ext_vector_type(4)));
typedef unsigned u32x2 __attribute__((ext_vector_type(2)));
typedef unsigned u32x3 __attribute__((ext_vector_type(3)));
typedef unsigned u32x4 __attribute__((ext_vector_type(4)));
#define LDS_AS __attribute__((address_space(3)))
#define DEVI __device__ __forceinline__

constexpr int S = 2048, D = 640, F = 1536;
constexpr float EPS = 1e-5f;

constexpr size_t WS_UT = 0;
constexpr size_t WS_XT = WS_UT + (size_t)D * S * 2;
constexpr size_t WS_HT = WS_XT + (size_t)D * S * 2;
constexpr size_t WS_ST = WS_HT + (size_t)D * S * 2;
constexpr size_t WS_WGU = WS_ST + (size_t)S * 8;
constexpr size_t WS_WD = WS_WGU + (size_t)2 * D * F * 2;
constexpr size_t WS_CS = WS_WD + (size_t)D * F * 2;
constexpr size_t WS_HH = WS_CS + (size_t)4 * 2 * F * 4;

DEVI float bf_lo(unsigned u) { return __builtin_bit_cast(float, u << 16); }
DEVI float bf_hi(unsigned u) { return __builtin_bit_cast(float, u & 0xffff0000u); }
DEVI unsigned pk_bf16(float a, float b) {
  f32x2 v = {a, b};
  bf16x2 r = __builtin_convertvector(v, bf16x2);
  return __builtin_bit_cast(unsigned, r);
}
DEVI float rnd_bf16(float a) { return bf_lo(pk_bf16(a, a)); }
DEVI s16x4 tr_read(unsigned lds_byte) {
  return __builtin_amdgcn_ds_read_tr16_b64_v4i16((LDS_AS s16x4*)(uintptr_t)lds_byte);
}
DEVI bf16x8 cat8(s16x4 a, s16x4 b) {
  s16x8 ab = __builtin_shufflevector(a, b, 0, 1, 2, 3, 4, 5, 6, 7);
  return __builtin_bit_cast(bf16x8, ab);
}
DEVI f32x4 mfma16(bf16x8 a, bf16x8 b, f32x4 c) { return __builtin_amdgcn_mfma_f32_16x16x32_bf16(a, b, c, 0, 0, 0); }
DEVI unsigned lds_addr(const void* p) { return (unsigned)(uintptr_t)(LDS_AS const char*)p; }
DEVI void glds16(const void* g, unsigned lds_wave_base) {
  __builtin_amdgcn_global_load_lds((const __attribute__((address_space(1))) void*)g,
                                   (LDS_AS void*)(uintptr_t)lds_wave_base, 16, 0, 0);
}
#define CBAR() asm volatile("" ::: "memory")
#define RAW_BAR() do { CBAR(); __builtin_amdgcn_s_barrier(); CBAR(); } while (0)

constexpr int K0_TR = 320, K0_ST = 128;
__global__ __launch_bounds__(256) void k0_prep(const u16* __restrict__ x, const u16* __restrict__ kin,
                                               const u16* __restrict__ vin, const u16* __restrict__ shk,
                                               char* __restrict__ ws) {
  __shared__ __attribute__((aligned(16))) char smem[2 * 64 * 144];
  const int tid = threadIdx.x, lane = tid & 63, w = tid >> 6;
  const int b = blockIdx.x;
  if (b < K0_TR) {
    const int tt = b & 31, cg = b >> 5;
    const int r2 = tid >> 3, cc = tid & 7;
    const int t0 = 64 * tt + 2 * r2, c0 = 64 * cg + 8 * cc;
    u32x4 kr[5], vr[2], xr[2], sw[4];
#pragma unroll
    for (int i = 0; i < 5; ++i) {
      const int t = t0 - 3 + i;
      const int tc = t < 0 ? 0 : t;
      kr[i] = *(const u32x4*)(kin + (size_t)tc * D + c0);
      if (t < 0) kr[i] = (u32x4){0, 0, 0, 0};
    }
#pragma unroll
    for (int i = 0; i < 2; ++i) {
      vr[i] = *(const u32x4*)(vin + (size_t)(t0 + i) * D + c0);
      xr[i] = *(const u32x4*)(x + (size_t)(t0 + i) * D + c0);
    }
#pragma unroll
    for (int i = 0; i < 4; ++i) sw[i] = *(const u32x4*)(shk + (size_t)c0 * 4 + 8 * i);
    char* su = smem;
    char* sx = smem + 64 * 144;
#pragma unroll
    for (int i = 0; i < 2; ++i) {
      u32x4 o;
#pragma unroll
      for (int cp = 0; cp < 4; ++cp) {
        float r[2];
#pragma unroll
        for (int hh = 0; hh < 2; ++hh) {
          const unsigned w01 = sw[cp][2 * hh], w23 = sw[cp][2 * hh + 1];
          float kk[4];
#pragma unroll
          for (int j = 0; j < 4; ++j) kk[j] = hh ? bf_hi(kr[i + 3 - j][cp]) : bf_lo(kr[i + 3 - j][cp]);
          float a = kk[3] * bf_hi(w23);
          a = fmaf(kk[2], bf_lo(w23), a);
          a = fmaf(kk[1], bf_hi(w01), a);
          a = fmaf(kk[0], bf_lo(w01), a);
          r[hh] = a;
        }
        const unsigned ks = pk_bf16(r[0], r[1]);
        o[cp] = pk_bf16(bf_lo(ks) * bf_lo(vr[i][cp]), bf_hi(ks) * bf_hi(vr[i][cp]));
      }
      *(u32x4*)(su + (2 * r2 + i) * 144 + cc * 16) = o;
      *(u32x4*)(sx + (2 * r2 + i) * 144 + cc * 16) = xr[i];
    }
    __syncthreads();
    const int g = lane >> 4, q = (lane >> 2) & 3, p = lane & 3, ci16 = lane & 15;
    const int c = 64 * cg + 16 * w + ci16;
    const unsigned colb = (16 * w + 4 * p) * 2;
    u16* uT = (u16*)(ws + WS_UT);
    u16* xT = (u16*)(ws + WS_XT);
#pragma unroll
    for (int si = 0; si < 2; ++si) {
      const int ci = 4 * si + g;
      {
        const int rho = ci >> 1, half = ci & 1;
        const int row = 4 * (8 * half + q) + rho;
        const unsigned a = lds_addr(su) + row * 144 + colb;
        const bf16x8 v = cat8(tr_read(a), tr_read(a + 16 * 144));
        *(bf16x8*)(uT + ((size_t)c * 32 + tt) * 64 + ci * 8) = v;
      }
      {
        const int row = 8 * ci + q;
        const unsigned a = lds_addr(sx) + row * 144 + colb;
        const bf16x8 v = cat8(tr_read(a), tr_read(a + 4 * 144));
        *(bf16x8*)(xT + (size_t)c * S + 64 * tt + ci * 8) = v;
      }
    }
  } else {
    const int row = (b - K0_TR) * 16 + 4 * w + (lane >> 4), sub = lane & 15;
    u32x4 xr[5];
#pragma unroll
    for (int i = 0; i < 5; ++i) xr[i] = *(const u32x4*)(x + (size_t)row * D + (sub + 16 * i) * 8);
    float s = 0.f;
#pragma unroll
    for (int i = 0; i < 5; ++i)
#pragma unroll
      for (int j = 0; j < 4; ++j) s += bf_lo(xr[i][j]) + bf_hi(xr[i][j]);
#pragma unroll
    for (int o = 1; o < 16; o <<= 1) s += __shfl_xor(s, o, 64);
    const float mean = s * (1.0f / D);
    float q = 0.f;
#pragma unroll
    for (int i = 0; i < 5; ++i)
#pragma unroll
      for (int j = 0; j < 4; ++j) {
        const float a = bf_lo(xr[i][j]) - mean, c = bf_hi(xr[i][j]) - mean;
        q = fmaf(a, a, q);
        q = fmaf(c, c, q);
      }
#pragma unroll
    for (int o = 1; o < 16; o <<= 1) q += __shfl_xor(q, o, 64);
    const float var = rnd_bf16(q * (1.0f / D));
    const float sd = rnd_bf16(sqrtf(rnd_bf16(var + EPS)));
    if (sub == 0) {
      f32x2 st = {rnd_bf16(mean), 1.0f / sd};
      *(f32x2*)(ws + WS_ST + (size_t)row * 8) = st;
    }
  }
}

constexpr int K1_CONV = 214, K1_GU = 96, K1_DN = 48;
constexpr int CH_U = 0, CH_K = 4608, CH_C = 4608 + 4160, CH_BYTES = CH_C + 8224;
constexpr int K1_LDS = 3 * CH_BYTES;

__global__ __launch_bounds__(768) void k1_conv(const u16* __restrict__ ssmk, const u16* __restrict__ ssmD,
                                               const u16* __restrict__ Wg, const u16* __restrict__ Wu,
                                               const u16* __restrict__ Wd, char* __restrict__ ws) {
  __shared__ __attribute__((aligned(16))) char smem[K1_LDS];
  const int tid = threadIdx.x, lane = tid & 63;
  const int w = __builtin_amdgcn_readfirstlane(tid >> 6);
  const int b = blockIdx.x;
  if (b < K1_CONV) {
    const int lc = w >> 2, p = w & 3;
    const int ch = 3 * b + lc;
    const bool live = ch < D;
    const int cthi = 7 - p, ctlo = p;
    const unsigned base = lds_addr(smem) + lc * CH_BYTES;
    const int a16 = lane & 15, g = lane >> 4;
    u32x2 xv[2];
    f32x4 st[2][2];
    float dval = 0.f;
    if (live) {
      const u16* uT = (const u16*)(ws + WS_UT);
      glds16(uT + (size_t)ch * S + (lane >> 1) * 64 + p * 16 + (lane & 1) * 8, base + CH_U + p * 1152 + 128);
      glds16(ssmk + (size_t)ch * S + p * 512 + lane * 8, base + CH_K + 32 + p * 1024);
      char* cb = smem + lc * CH_BYTES;
      if (lane < 8) *(u32x4*)(cb + CH_U + p * 1152 + lane * 16) = (u32x4){0, 0, 0, 0};
      if (p == 0 && lane < 2) *(u32x4*)(cb + CH_K + lane * 16) = (u32x4){0, 0, 0, 0};
      if (p == 0 && lane == 2) *(u32x4*)(cb + CH_C) = (u32x4){0, 0, 0, 0};
      const u16* xT = (const u16*)(ws + WS_XT);
#pragma unroll
      for (int i = 0; i < 2; ++i) {
        const int s0 = 256 * (i ? ctlo : cthi) + 16 * a16 + 4 * g;
        xv[i] = *(const u32x2*)(xT + (size_t)ch * S + s0);
        st[i][0] = *(const f32x4*)(ws + WS_ST + (size_t)s0 * 8);
        st[i][1] = *(const f32x4*)(ws + WS_ST + (size_t)s0 * 8 + 16);
      }
      dval = bf_lo(ssmD[ch]);
    }
    __syncthreads();
    f32x4 ahi[4], alo[4];
#pragma unroll
    for (int r = 0; r < 4; ++r) { ahi[r] = (f32x4){0, 0, 0, 0}; alo[r] = (f32x4){0, 0, 0, 0}; }
    if (live) {
      unsigned ab = base + CH_U + 128 + 2 * (4 * a16 - 4 - 8 * g) + 64 * (2 * cthi);
      const int q = (lane >> 2) & 3, pp = lane & 3;
      unsigned aa = base + CH_K + 32 + 2 * (32 * g - 4 * q + 4 * pp);
      const int n1 = 2 * (cthi - ctlo);
      for (int n = 0; n < n1; ++n) {
        const bf16x8 A = cat8(tr_read(aa + 32), tr_read(aa));
#pragma unroll
        for (int r = 0; r < 4; ++r) {
          const s16x4 b0 = *(LDS_AS const s16x4*)(uintptr_t)(ab + r * 1152);
          const s16x4 b1 = *(LDS_AS const s16x4*)(uintptr_t)(ab + r * 1152 + 8);
          ahi[r] = mfma16(A, cat8(b0, b1), ahi[r]);
        }
        aa += 256;
        ab -= 64;
      }
      const int n2 = 2 * ctlo + 2;
      unsigned al = aa - 256 * n1;
      for (int n = 0; n < n2; ++n) {
        const bf16x8 A = cat8(tr_read(aa + 32), tr_read(aa));
        const bf16x8 A2 = cat8(tr_read(al + 32), tr_read(al));
#pragma unroll
        for (int r = 0; r < 4; ++r) {
          const s16x4 b0 = *(LDS_AS const s16x4*)(uintptr_t)(ab + r * 1152);
          const s16x4 b1 = *(LDS_AS const s16x4*)(uintptr_t)(ab + r * 1152 + 8);
          const bf16x8 B = cat8(b0, b1);
          ahi[r] = mfma16(A, B, ahi[r]);
          alo[r] = mfma16(A2, B, alo[r]);
        }
        aa += 256;
        al += 256;
        ab -= 64;
      }
      char* cb = smem + lc * CH_BYTES;
#pragma unroll
      for (int i = 0; i < 2; ++i) {
        const f32x4* z = i ? alo : ahi;
        const int blk = 64 * (i ? ctlo : cthi) + 4 * a16 + g;
        f32x4 c;
        c[0] = z[1][3] + z[2][2] + z[3][1];
        c[1] = z[2][3] + z[3][2];
        c[2] = z[3][3];
        c[3] = 0.f;
        *(f32x4*)(cb + CH_C + (blk + 1) * 16) = c;
      }
    }
    __syncthreads();
    if (live) {
      char* cb = smem + lc * CH_BYTES;
      u16* hT = (u16*)(ws + WS_HT);
#pragma unroll
      for (int i = 0; i < 2; ++i) {
        const f32x4* z = i ? alo : ahi;
        const int blk = 64 * (i ? ctlo : cthi) + 4 * a16 + g;
        const f32x4 c = *(const f32x4*)(cb + CH_C + blk * 16);
        float y[4];
        y[0] = z[0][0] + c[0];
        y[1] = z[0][1] + z[1][0] + c[1];
        y[2] = z[0][2] + z[1][1] + z[2][0] + c[2];
        y[3] = z[0][3] + z[1][2] + z[2][1] + z[3][0];
        float h[4];
#pragma unroll
        for (int e = 0; e < 4; ++e) {
          const float uu = bf_lo(*(const u16*)(cb + CH_U + e * 1152 + 128 + blk * 2));
          const float xx = (e & 1) ? bf_hi(xv[i][e >> 1]) : bf_lo(xv[i][e >> 1]);
          const float m = st[i][e >> 1][2 * (e & 1)], rs = st[i][e >> 1][2 * (e & 1) + 1];
          const float y2 = rnd_bf16(rnd_bf16(y[e]) + rnd_bf16(uu * dval));
          const float qn = rnd_bf16(rnd_bf16(xx - m) * rs);
          h[e] = xx + rnd_bf16(qn * y2);
        }
        u32x2 o = {pk_bf16(h[0], h[1]), pk_bf16(h[2], h[3])};
        *(u32x2*)(hT + (size_t)ch * S + 4 * blk) = o;
      }
    }
  } else if (b < K1_CONV + K1_GU) {
    const int u = b - K1_CONV, fb = u >> 2, kq = u & 3;
    constexpr int RS = 272;
    for (int i = tid; i < 160 * 8; i += 768) {
      const int k = i >> 3, c8 = i & 7;
      const u32x4 gv = *(const u32x4*)(Wg + (size_t)(160 * kq + k) * F + 64 * fb + 8 * c8);
      const u32x4 uv = *(const u32x4*)(Wu + (size_t)(160 * kq + k) * F + 64 * fb + 8 * c8);
      *(u32x4*)(smem + k * RS + c8 * 32) = (u32x4){gv[0], uv[0], gv[1], uv[1]};
      *(u32x4*)(smem + k * RS + c8 * 32 + 16) = (u32x4){gv[2], uv[2], gv[3], uv[3]};
    }
    __syncthreads();
    if (w < 8) {
      const int g = lane >> 4, q = (lane >> 2) & 3, p = lane & 3;
      const int T = 8 * fb + w;
      const bf16x8 ones = __builtin_bit_cast(bf16x8, (u32x4){0x3f803f80u, 0x3f803f80u, 0x3f803f80u, 0x3f803f80u});
      f32x4 cs = {0, 0, 0, 0};
      char* dst = ws + WS_WGU + ((size_t)(T / 3) * 60 + (T % 3)) * 1024 + lane * 16;
#pragma unroll
      for (int s = 0; s < 5; ++s) {
        const unsigned a = lds_addr(smem) + (32 * s + 4 * g + q) * RS + w * 32 + p * 8;
        const bf16x8 v = cat8(tr_read(a), tr_read(a + 16 * RS));
        cs = mfma16(v, ones, cs);
        *(bf16x8*)(dst + (size_t)(5 * kq + s) * 3 * 1024) = v;
      }
      if ((lane & 15) == 0) *(f32x4*)(ws + WS_CS + ((size_t)kq * 192 + T) * 64 + g * 16) = cs;
    }
  } else {
    const int ks2 = b - K1_CONV - K1_GU;
    constexpr int RS = 1280 + 32;
    for (int i = tid; i < 32 * 80; i += 768) {
      const int k = i / 80, c8 = i % 80;
      *(u32x4*)(smem + k * RS + c8 * 16) = *(const u32x4*)(Wd + (size_t)(32 * ks2 + k) * D + 8 * c8);
    }
    __syncthreads();
    const int g = lane >> 4, q = (lane >> 2) & 3, p = lane & 3;
    for (int dt = w; dt < 40; dt += 12) {
      const int f = 8 * (q >> 1) + 2 * g + (q & 1);
      const unsigned a = lds_addr(smem) + f * RS + dt * 32 + p * 8;
      const bf16x8 v = cat8(tr_read(a), tr_read(a + 16 * RS));
      *(bf16x8*)(ws + WS_WD + (((size_t)(dt / 5) * 48 + ks2) * 5 + (dt % 5)) * 1024 + lane * 16) = v;
    }
  }
}

constexpr int K2_WIN = 4;
__global__ __launch_bounds__(320) void k2_gemm1(char* __restrict__ ws) {
  __shared__ __attribute__((aligned(16))) char smem[163840];
  const int tid = threadIdx.x, lane = tid & 63;
  const int w = __builtin_amdgcn_readfirstlane(tid >> 6);
  const int b = blockIdx.x, xcd = b & 7, r = b >> 3;
  const int tb = 8 * (xcd & 1) + (r & 7), nb = 4 * (xcd >> 1) + (r >> 3);
  const unsigned sbase = lds_addr(smem);
  if (w == 4) {
    const u16* hT = (const u16*)(ws + WS_HT);
    const int rr = lane >> 4, cp = lane & 15;
    const u16* src0 = hT + (size_t)rr * S + 128 * tb + 8 * (cp ^ (2 * rr));
    const u16* src1 = hT + (size_t)(rr + 4) * S + 128 * tb + 8 * (cp ^ (2 * (rr + 4)));
    auto issue = [&](int c) {
#pragma unroll
      for (int i = 0; i < 8; ++i) {
        glds16(src0 + (size_t)(64 * c + 8 * i) * S, sbase + (64 * c + 8 * i) * 256);
        glds16(src1 + (size_t)(64 * c + 8 * i) * S, sbase + (64 * c + 8 * i + 4) * 256);
      }
    };
    issue(0); issue(1); issue(2);
    for (int c = 0; c < 7; ++c) {
      asm volatile("s_waitcnt vmcnt(32)" ::: "memory");
      RAW_BAR();
      issue(c + 3);
    }
    asm volatile("s_waitcnt vmcnt(32)" ::: "memory"); RAW_BAR();
    asm volatile("s_waitcnt vmcnt(16)" ::: "memory"); RAW_BAR();
    asm volatile("s_waitcnt vmcnt(0)" ::: "memory"); RAW_BAR();
    RAW_BAR();
    return;
  }
  const int g = lane >> 4, q = (lane >> 2) & 3, p = lane & 3, t16 = lane & 15;
  const char* wsrc = ws + WS_WGU + (size_t)(nb * 4 + w) * 60 * 1024 + lane * 16;
  bf16x8 wf[K2_WIN][3];
#pragma unroll
  for (int s = 0; s < K2_WIN; ++s)
#pragma unroll
    for (int j = 0; j < 3; ++j) wf[s][j] = *(const bf16x8*)(wsrc + (s * 3 + j) * 1024);
  unsigned ta[8];
  {
    const unsigned lb = sbase + (4 * g + q) * 256 + 32 * (4 * (g & 1) + q) + 16 * (p >> 1) + 8 * (p & 1);
#pragma unroll
    for (int i = 0; i < 8; ++i) ta[i] = lb ^ (32 * ((i + 2 * w) & 7));
  }
  f32x4 acc[3][8], sacc[2], gacc[2];
#pragma unroll
  for (int j = 0; j < 3; ++j)
#pragma unroll
    for (int i = 0; i < 8; ++i) acc[j][i] = (f32x4){0, 0, 0, 0};
  sacc[0] = sacc[1] = gacc[0] = gacc[1] = (f32x4){0, 0, 0, 0};
  const bf16x8 ones = __builtin_bit_cast(bf16x8, (u32x4){0x3f803f80u, 0x3f803f80u, 0x3f803f80u, 0x3f803f80u});
  for (int it = 0; it < 5; ++it) {
#pragma unroll
    for (int s = 0; s < 4; ++s) {
      if ((s & 1) == 0) RAW_BAR();
      bf16x8 af[8];
#pragma unroll
      for (int i = 0; i < 8; ++i) af[i] = cat8(tr_read(ta[i] + s * 8192), tr_read(ta[i] + s * 8192 + 4096));
      bf16x8 wc[3];
#pragma unroll
      for (int j = 0; j < 3; ++j) wc[j] = wf[s][j];
      if (it < 4) {
#pragma unroll
        for (int j = 0; j < 3; ++j) wf[s][j] = *(const bf16x8*)(wsrc + ((4 * it + s + K2_WIN) * 3 + j) * 1024);
      }
#pragma unroll
      for (int i = 0; i < 8; ++i) {
#pragma unroll
        for (int j = 0; j < 3; ++j) acc[j][i] = mfma16(wc[j], af[i], acc[j][i]);
        if (i < 2) {
          sacc[i] = mfma16(ones, af[i], sacc[i]);
          gacc[i] = mfma16(af[i], af[i], gacc[i]);
        }
      }
    }
#pragma unroll
    for (int i = 0; i < 8; ++i) ta[i] += 32768;
  }
  f32x4 cs[3];
#pragma unroll
  for (int j = 0; j < 3; ++j) {
    const int T = nb * 12 + w * 3 + j;
    cs[j] = *(const f32x4*)(ws + WS_CS + (size_t)T * 64 + g * 16);
#pragma unroll
    for (int kq = 1; kq < 4; ++kq) cs[j] += *(const f32x4*)(ws + WS_CS + ((size_t)kq * 192 + T) * 64 + g * 16);
  }
#pragma unroll
  for (int i = 0; i < 2; ++i) {
    const float sm = sacc[i][0];
    const int e = t16 & 3;
    const float sq = e == 0 ? gacc[i][0] : e == 1 ? gacc[i][1] : e == 2 ? gacc[i][2] : gacc[i][3];
    if (g == (t16 >> 2)) {
      const float mean = sm * (1.0f / D);
      float var = fmaf(-mean, mean, sq * (1.0f / D));
      var = rnd_bf16(fmaxf(var, 0.f));
      const float sd = rnd_bf16(sqrtf(rnd_bf16(var + EPS)));
      f32x2 st = {rnd_bf16(mean), 1.0f / sd};
      *(f32x2*)(smem + ((2 * w + i) * 16 + t16) * 8) = st;
    }
  }
  asm volatile("s_waitcnt lgkmcnt(0)" ::: "memory");
  RAW_BAR();
  const int T0 = nb * 12 + w * 3;
#pragma unroll
  for (int i = 0; i < 8; ++i) {
    const int tt = (i + 2 * w) & 7;
    const f32x2 st = *(const f32x2*)(smem + (tt * 16 + t16) * 8);
    char* dst = ws + WS_HH + (size_t)(tb * 8 + tt) * 48 * 1024 + lane * 16;
#pragma unroll
    for (int j = 0; j < 3; ++j) {
      const float g0 = rnd_bf16((acc[j][i][0] - st[0] * cs[j][0]) * st[1]);
      const float g1 = rnd_bf16((acc[j][i][1] - st[0] * cs[j][1]) * st[1]);
      const float u0 = rnd_bf16((acc[j][i][2] - st[0] * cs[j][2]) * st[1]);
      const float u1 = rnd_bf16((acc[j][i][3] - st[0] * cs[j][3]) * st[1]);
      const unsigned o = pk_bf16(g0 > 0.f ? g0 * u0 : 0.f, g1 > 0.f ? g1 * u1 : 0.f);
      const int T = T0 + j;
      *(unsigned*)(dst + (T >> 2) * 1024 + (T & 3) * 4) = o;
    }
  }
}

constexpr int K3_WIN = 4;
__global__ __launch_bounds__(256) void k3_gemm2(const char* __restrict__ ws, u16* __restrict__ out) {
  __shared__ __attribute__((aligned(16))) char smem[4 * 15 * 1024];
  const int tid = threadIdx.x, lane = tid & 63;
  const int w = __builtin_amdgcn_readfirstlane(tid >> 6);
  const int b = blockIdx.x, xcd = b & 7, r = b >> 3;
  const int tb = 8 * (xcd & 3) + (r & 7), db = 4 * (xcd >> 2) + (r >> 3);
  const int g = lane >> 4, t16 = lane & 15;
  const char* wsrc = ws + WS_WD + ((size_t)db * 48 + 12 * w) * 5 * 1024 + lane * 16;
  const char* hsrc = ws + WS_HH + ((size_t)tb * 4 * 48 + 12 * w) * 1024 + lane * 16;
  bf16x8 wf[K3_WIN][5], hf[K3_WIN][4];
#pragma unroll
  for (int s = 0; s < K3_WIN; ++s) {
#pragma unroll
    for (int j = 0; j < 5; ++j) wf[s][j] = *(const bf16x8*)(wsrc + (s * 5 + j) * 1024);
#pragma unroll
    for (int i = 0; i < 4; ++i) hf[s][i] = *(const bf16x8*)(hsrc + (((i + w) & 3) * 48 + s) * 1024);
  }
  const u16* hT = (const u16*)(ws + WS_HT);
  u16 hres[5][4];
#pragma unroll
  for (int j = 0; j < 5; ++j)
#pragma unroll
    for (int e = 0; e < 4; ++e) hres[j][e] = hT[(size_t)(80 * db + 16 * j + 4 * g + e) * S + 64 * tb + 16 * w + t16];
  f32x4 acc[4][5];
#pragma unroll
  for (int i = 0; i < 4; ++i)
#pragma unroll
    for (int j = 0; j < 5; ++j) acc[i][j] = (f32x4){0, 0, 0, 0};
  for (int it = 0; it < 3; ++it) {
#pragma unroll
    for (int s = 0; s < 4; ++s) {
      bf16x8 wc[5], hc[4];
#pragma unroll
      for (int j = 0; j < 5; ++j) wc[j] = wf[s][j];
#pragma unroll
      for (int i = 0; i < 4; ++i) hc[i] = hf[s][i];
      if (it < 2) {
        const int ks = 4 * it + s + K3_WIN;
#pragma unroll
        for (int j = 0; j < 5; ++j) wf[s][j] = *(const bf16x8*)(wsrc + (ks * 5 + j) * 1024);
#pragma unroll
        for (int i = 0; i < 4; ++i) hf[s][i] = *(const bf16x8*)(hsrc + (((i + w) & 3) * 48 + ks) * 1024);
      }
#pragma unroll
      for (int i = 0; i < 4; ++i)
#pragma unroll
        for (int j = 0; j < 5; ++j) acc[i][j] = mfma16(wc[j], hc[i], acc[i][j]);
    }
  }
#pragma unroll
  for (int i = 1; i < 4; ++i) {
    const int owner = (i + w) & 3;
#pragma unroll
    for (int j = 0; j < 5; ++j) *(f32x4*)(smem + ((owner * 3 + (i - 1)) * 5 + j) * 1024 + lane * 16) = acc[i][j];
  }
  __syncthreads();
#pragma unroll
  for (int j = 0; j < 5; ++j) {
    f32x4 part[4];
#pragma unroll
    for (int i = 1; i < 4; ++i) part[i] = *(const f32x4*)(smem + ((w * 3 + (i - 1)) * 5 + j) * 1024 + lane * 16);
    const f32x4 o = (acc[0][j] + part[1]) + (part[2] + part[3]);
    float res[4];
#pragma unroll
    for (int e = 0; e < 4; ++e) res[e] = bf_lo(hres[j][e]) + rnd_bf16(o[e]);
    u32x2 ov = {pk_bf16(res[0], res[1]), pk_bf16(res[2], res[3])};
    *(u32x2*)(out + (size_t)(64 * tb + 16 * w + t16) * D + 80 * db + 16 * j + 4 * g) = ov;
  }
}

extern "C" void kernel_launch(void* const* d_in, const int* in_sizes, int n_in, void* d_out, int out_size,
                              void* d_ws, size_t ws_size, hipStream_t stream) {
  const u16* q = (const u16*)d_in[0];
  const u16* k = (const u16*)d_in[1];
  const u16* v = (const u16*)d_in[2];
  const u16* shk = (const u16*)d_in[3];
  const u16* ssmk = (const u16*)d_in[4];
  const u16* ssmD = (const u16*)d_in[5];
  const u16* Wg = (const u16*)d_in[7];
  const u16* Wu = (const u16*)d_in[8];
  const u16* Wd = (const u16*)d_in[9];
  char* ws = (char*)d_ws;
  k0_prep<<<K0_TR + K0_ST, 256, 0, stream>>>(q, k, v, shk, ws);
  k1_conv<<<K1_CONV + K1_GU + K1_DN, 768, 0, stream>>>(ssmk, ssmD, Wg, Wu, Wd, ws);
  k2_gemm1<<<256, 320, 0, stream>>>(ws);
  k3_gemm2<<<256, 256, 0, stream>>>(ws, (u16*)d_out);
}
```

```cpp
#include <hip/hip_runtime.h>
#include <stdint.h>

typedef unsigned short u16;
typedef short s16x4 __attribute__((ext_vector_type(4)));
typedef short s16x8 __attribute__((ext_vector_type(8)));
typedef __bf16 bf16x8 __attribute__((ext_vector_type(8)));
typedef __bf16 bf16x2 __attribute__((ext_vector_type(2)));
typedef float f32x2 __attribute__((ext_vector_type(2)));
typedef float f32x4 __attribute__((ext_vector_type(4)));
typedef unsigned u32x2 __attribute__((ext_vector_type(2)));
typedef unsigned u32x4 __attribute__((ext_vector_type(4)));
#define LDS_AS __attribute__((address_space(3)))
#define DEVI __device__ __forceinline__

constexpr int S = 2048, D = 640, F = 1536;
constexpr float EPS = 1e-5f;
constexpr unsigned MARK = 0xffffffffu;
constexpr int SPIN_CAP = 1 << 14;

constexpr unsigned WS_UT = 0;
constexpr unsigned WS_XT = WS_UT + D * S * 2;
constexpr unsigned WS_HT = WS_XT + D * S * 2;
constexpr unsigned WS_ST = WS_HT + D * S * 2;
constexpr unsigned WS_WGU = WS_ST + S * 8;
constexpr unsigned WS_WD = WS_WGU + 2 * D * F * 2;
constexpr unsigned WS_CS = WS_WD + D * F * 2;
constexpr unsigned WS_HH = WS_CS + 5 * 2 * F * 4;
constexpr unsigned WS_END = WS_HH + S * F * 2;

DEVI float bf_lo(unsigned u) { return __builtin_bit_cast(float, u << 16); }
DEVI float bf_hi(unsigned u) { return __builtin_bit_cast(float, u & 0xffff0000u); }
DEVI unsigned pk_bf16(float a, float b) {
  f32x2 v = {a, b};
  bf16x2 r = __builtin_convertvector(v, bf16x2);
  return __builtin_bit_cast(unsigned, r);
}
DEVI float rnd_bf16(float a) { return bf_lo(pk_bf16(a, a)); }
DEVI unsigned unmark(unsigned u) { return u < 0xfffffffeu ? u : 0xfffffffeu; }
DEVI s16x4 tr_read(unsigned lds_byte) {
  return __builtin_amdgcn_ds_read_tr16_b64_v4i16((LDS_AS s16x4*)(uintptr_t)lds_byte);
}
DEVI s16x4 lds_r64(unsigned a) { return *(LDS_AS const s16x4*)(uintptr_t)a; }
DEVI u32x4 lds_r128(unsigned a) { return *(LDS_AS const u32x4*)(uintptr_t)a; }
DEVI void lds_w128(unsigned a, u32x4 v) { *(LDS_AS u32x4*)(uintptr_t)a = v; }
DEVI f32x4 lds_r128f(unsigned a) { return *(LDS_AS const f32x4*)(uintptr_t)a; }
DEVI void lds_w128f(unsigned a, f32x4 v) { *(LDS_AS f32x4*)(uintptr_t)a = v; }
DEVI bf16x8 cat8(s16x4 a, s16x4 b) {
  s16x8 ab = __builtin_shufflevector(a, b, 0, 1, 2, 3, 4, 5, 6, 7);
  return __builtin_bit_cast(bf16x8, ab);
}
DEVI bf16x8 as_bf(u32x4 v) { return __builtin_bit_cast(bf16x8, v); }
DEVI f32x4 mfma16(bf16x8 a, bf16x8 b, f32x4 c) { return __builtin_amdgcn_mfma_f32_16x16x32_bf16(a, b, c, 0, 0, 0); }
DEVI unsigned lds_addr(const void* p) { return (unsigned)(uintptr_t)(LDS_AS const char*)p; }
DEVI void glds16_asm(const void* gsrc, unsigned lds_dst) {
  unsigned keep;
  asm volatile("s_mov_b32 %0, m0\n\ts_mov_b32 m0, %2\n\ts_nop 0\n\tglobal_load_lds_dwordx4 %1, off\n\ts_mov_b32 m0, %0"
               : "=&s"(keep) : "v"(gsrc), "s"(lds_dst) : "memory");
}
typedef __amdgpu_buffer_rsrc_t rsrc_t;
DEVI u32x4 ld_dev(rsrc_t rs, unsigned voff, unsigned soff) { return __builtin_amdgcn_raw_buffer_load_b128(rs, voff, soff, 16); }
DEVI u32x4 ld_pl(rsrc_t rs, unsigned voff, unsigned soff) { return __builtin_amdgcn_raw_buffer_load_b128(rs, voff, soff, 0); }
DEVI void st_dev(rsrc_t rs, unsigned voff, unsigned soff, u32x4 v) { __builtin_amdgcn_raw_buffer_store_b128(v, rs, voff, soff, 16); }
DEVI unsigned max4(u32x4 v) { return max(max(v[0], v[1]), max(v[2], v[3])); }
template <int CTRL> DEVI float dpp_f(float x) {
  return __builtin_bit_cast(float, __builtin_amdgcn_mov_dpp(__builtin_bit_cast(int, x), CTRL, 0xf, 0xf, true));
}
DEVI float f_of(unsigned u) { return __builtin_bit_cast(float, u); }
DEVI unsigned u_of(float f) { return __builtin_bit_cast(unsigned, f); }

#define STAMP(k, i) do {} while (0)
#define CBAR() asm volatile("" ::: "memory")
#define RAW_BAR() do { CBAR(); __builtin_amdgcn_s_barrier(); CBAR(); } while (0)
#define LGKM0_BAR() do { asm volatile("s_waitcnt lgkmcnt(0)" ::: "memory"); __builtin_amdgcn_s_barrier(); CBAR(); } while (0)
#define SCHED() __builtin_amdgcn_sched_barrier(0)

constexpr int K0_TR = 320, K0_ST = 128, K0_GU = 120, K0_DN = 60;
__global__ __launch_bounds__(256) void k0_prep(const u16* __restrict__ x, const u16* __restrict__ kin,
                                               const u16* __restrict__ vin, const u16* __restrict__ shk,
                                               const u16* __restrict__ Wg, const u16* __restrict__ Wu,
                                               const u16* __restrict__ Wd, char* __restrict__ ws) {
  __shared__ __attribute__((aligned(16))) char smem[4 * 32 * 288 + 2048];
  const int tid = threadIdx.x, lane = tid & 63;
  const int w = __builtin_amdgcn_readfirstlane(tid >> 6);
  const int b = blockIdx.x;
  const int g = lane >> 4, q = (lane >> 2) & 3, p = lane & 3;
  STAMP(0, 0);
  if (b < K0_TR) {
    const int tt = b & 31, cg = b >> 5;
    const int r2 = tid >> 3, cc = tid & 7;
    const int t0 = 64 * tt + 2 * r2, c0 = 64 * cg + 8 * cc;
    u32x4 kr[5], vr[2], xr[2], sw[4];
#pragma unroll
    for (int i = 0; i < 5; ++i) {
      const int t = t0 - 3 + i;
      const int tc = t < 0 ? 0 : t;
      kr[i] = *(const u32x4*)(kin + (size_t)tc * D + c0);
      if (t < 0) kr[i] = (u32x4){0, 0, 0, 0};
    }
#pragma unroll
    for (int i = 0; i < 2; ++i) {
      vr[i] = *(const u32x4*)(vin + (size_t)(t0 + i) * D + c0);
      xr[i] = *(const u32x4*)(x + (size_t)(t0 + i) * D + c0);
    }
#pragma unroll
    for (int i = 0; i < 4; ++i) sw[i] = *(const u32x4*)(shk + (size_t)c0 * 4 + 8 * i);
#pragma unroll
    for (int i = 0; i < 2; ++i) *(u32x4*)(ws + WS_HT + (size_t)b * 8192 + i * 4096 + tid * 16) = (u32x4){MARK, MARK, MARK, MARK};
    char* su = smem;
    char* sx = smem + 64 * 144;
#pragma unroll
    for (int i = 0; i < 2; ++i) {
      u32x4 o;
#pragma unroll
      for (int cp = 0; cp < 4; ++cp) {
        float r[2];
#pragma unroll
        for (int hh = 0; hh < 2; ++hh) {
          const unsigned w01 = sw[cp][2 * hh], w23 = sw[cp][2 * hh + 1];
          float kk[4];
#pragma unroll
          for (int j = 0; j < 4; ++j) kk[j] = hh ? bf_hi(kr[i + 3 - j][cp]) : bf_lo(kr[i + 3 - j][cp]);
          float a = kk[3] * bf_hi(w23);
          a = fmaf(kk[2], bf_lo(w23), a);
          a = fmaf(kk[1], bf_hi(w01), a);
          a = fmaf(kk[0], bf_lo(w01), a);
          r[hh] = a;
        }
        const unsigned ks = pk_bf16(r[0], r[1]);
        o[cp] = pk_bf16(bf_lo(ks) * bf_lo(vr[i][cp]), bf_hi(ks) * bf_hi(vr[i][cp]));
      }
      *(u32x4*)(su + (2 * r2 + i) * 144 + cc * 16) = o;
      *(u32x4*)(sx + (2 * r2 + i) * 144 + cc * 16) = xr[i];
    }
    STAMP(0, 1);
    __syncthreads();
    STAMP(0, 2);
    const int c = 64 * cg + 16 * w + (lane & 15);
    const unsigned colb = (16 * w + 4 * p) * 2;
    u16* uT = (u16*)(ws + WS_UT);
    u16* xT = (u16*)(ws + WS_XT);
#pragma unroll
    for (int si = 0; si < 2; ++si) {
      const int ci = 4 * si + g;
      {
        const int rho = ci >> 1, half = ci & 1;
        const int row = 4 * (8 * half + q) + rho;
        const unsigned a = lds_addr(su) + row * 144 + colb;
        const bf16x8 v = cat8(tr_read(a), tr_read(a + 16 * 144));
        *(bf16x8*)(uT + ((size_t)c * 32 + tt) * 64 + ci * 8) = v;
      }
      {
        const int row = 8 * ci + q;
        const unsigned a = lds_addr(sx) + row * 144 + colb;
        const bf16x8 v = cat8(tr_read(a), tr_read(a + 4 * 144));
        *(bf16x8*)(xT + (size_t)c * S + 64 * tt + ci * 8) = v;
      }
    }
  } else if (b < K0_TR + K0_ST) {
    const int row = (b - K0_TR) * 16 + 4 * w + (lane >> 4), sub = lane & 15;
    u32x4 xr[5];
#pragma unroll
    for (int i = 0; i < 5; ++i) xr[i] = *(const u32x4*)(x + (size_t)row * D + (sub + 16 * i) * 8);
    float s = 0.f;
#pragma unroll
    for (int i = 0; i < 5; ++i)
#pragma unroll
      for (int j = 0; j < 4; ++j) s += bf_lo(xr[i][j]) + bf_hi(xr[i][j]);
    s += dpp_f<0xB1>(s); s += dpp_f<0x4E>(s); s += dpp_f<0x124>(s); s += dpp_f<0x128>(s);
    const float mean = s * (1.0f / D);
    float qq = 0.f;
#pragma unroll
    for (int i = 0; i < 5; ++i)
#pragma unroll
      for (int j = 0; j < 4; ++j) {
        const float a = bf_lo(xr[i][j]) - mean, c = bf_hi(xr[i][j]) - mean;
        qq = fmaf(a, a, qq);
        qq = fmaf(c, c, qq);
      }
    qq += dpp_f<0xB1>(qq); qq += dpp_f<0x4E>(qq); qq += dpp_f<0x124>(qq); qq += dpp_f<0x128>(qq);
    const float var = rnd_bf16(qq * (1.0f / D));
    const float sd = rnd_bf16(sqrtf(rnd_bf16(var + EPS)));
    if (sub == 0) {
      f32x2 st = {rnd_bf16(mean), 1.0f / sd};
      *(f32x2*)(ws + WS_ST + (size_t)row * 8) = st;
    }
  } else if (b < K0_TR + K0_ST + K0_GU) {
    const int u = b - K0_TR - K0_ST, fb = u / 5, kq = u % 5, ks = 4 * kq + w;
    constexpr int RS = 288;
    char* sm = smem + w * 32 * RS;
    u32x4 gv[4], uv[4];
#pragma unroll
    for (int i = 0; i < 4; ++i) {
      const size_t off = (size_t)(32 * ks + 8 * i + (lane >> 3)) * F + 64 * fb + 8 * (lane & 7);
      gv[i] = *(const u32x4*)(Wg + off);
      uv[i] = *(const u32x4*)(Wu + off);
    }
#pragma unroll
    for (int i = 0; i < 4; ++i) {
      char* d = sm + (8 * i + (lane >> 3)) * RS + (lane & 7) * 32;
      *(u32x4*)d = (u32x4){gv[i][0], uv[i][0], gv[i][1], uv[i][1]};
      *(u32x4*)(d + 16) = (u32x4){gv[i][2], uv[i][2], gv[i][3], uv[i][3]};
    }
    asm volatile("s_waitcnt lgkmcnt(0)" ::: "memory");
    const bf16x8 ones = as_bf((u32x4){0x3f803f80u, 0x3f803f80u, 0x3f803f80u, 0x3f803f80u});
    float* csl = (float*)(smem + 4 * 32 * RS);
#pragma unroll
    for (int t = 0; t < 8; ++t) {
      const int T = 8 * fb + t;
      const unsigned a = lds_addr(sm) + (4 * g + q) * RS + t * 32 + p * 8;
      const bf16x8 v = cat8(tr_read(a), tr_read(a + 16 * RS));
      const f32x4 cs = mfma16(v, ones, (f32x4){0, 0, 0, 0});
      *(bf16x8*)(ws + WS_WGU + ((size_t)(T / 3) * 60 + ks * 3 + (T % 3)) * 1024 + lane * 16) = v;
      if ((lane & 15) == 0) *(f32x4*)(csl + w * 128 + t * 16 + g * 4) = cs;
    }
    __syncthreads();
    if (tid < 128)
      *(float*)(ws + WS_CS + ((size_t)kq * 3072 + fb * 128 + tid) * 4) =
          (csl[tid] + csl[128 + tid]) + (csl[256 + tid] + csl[384 + tid]);
  } else {
    const int u = 4 * (b - K0_TR - K0_ST - K0_GU) + w, ks2 = u / 5, dq = u % 5;
    constexpr int RS = 288;
    char* sm = smem + w * 32 * RS;
    u32x4 v8[8];
#pragma unroll
    for (int i = 0; i < 8; ++i) v8[i] = *(const u32x4*)(Wd + (size_t)(32 * ks2 + 4 * i + g) * D + 128 * dq + 8 * (lane & 15));
#pragma unroll
    for (int i = 0; i < 8; ++i) *(u32x4*)(sm + (4 * i + g) * RS + (lane & 15) * 16) = v8[i];
    asm volatile("s_waitcnt lgkmcnt(0)" ::: "memory");
#pragma unroll
    for (int t = 0; t < 8; ++t) {
      const int dt = 8 * dq + t;
      const unsigned a = lds_addr(sm) + (8 * g + q) * RS + t * 32 + p * 8;
      const bf16x8 v = cat8(tr_read(a), tr_read(a + 4 * RS));
      *(bf16x8*)(ws + WS_WD + (((size_t)(dt / 5) * 48 + ks2) * 5 + (dt % 5)) * 1024 + lane * 16) = v;
    }
  }
  STAMP(0, 3);
}

constexpr int NCONV = 214;
constexpr int CH_U = 0, CH_K = 4608, CH_Y = CH_K + 4160, CH_C = CH_Y + 8192, CH_BYTES = CH_C + 8224;
constexpr int L_RES = 131072, L_ST = L_RES + 10240, L_CS = L_ST + 1024, L_TR = 143360;
constexpr int W2 = 6;

template <int TOP, int NT, bool WITH0>
DEVI void conv_run(unsigned base, int lane) {
  const int a16 = lane & 15, g = lane >> 4, q = (lane >> 2) & 3, pp = lane & 3;
  const unsigned ab = base + CH_U + 128 + 2 * (4 * a16 - 4 - 8 * g) - 64;
  const unsigned aa = base + CH_K + 32 + 2 * (32 * g - 4 * q + 4 * pp);
  constexpr int NTT = NT + (WITH0 ? 1 : 0);
  constexpr int NS = 2 * TOP + 2;
  constexpr int WN = 2 * NT;
  f32x4 acc[NTT][4];
#pragma unroll
  for (int t = 0; t < NTT; ++t)
#pragma unroll
    for (int r = 0; r < 4; ++r) acc[t][r] = (f32x4){0, 0, 0, 0};
  bf16x8 A[WN], B[2][4], A0[2];
  auto loadA = [&](int n) { return cat8(tr_read(aa + 256 * n + 32), tr_read(aa + 256 * n)); };
  auto loadB = [&](int st, bf16x8 (&dst)[4]) {
    const int xi = 2 * TOP - st;
#pragma unroll
    for (int r = 0; r < 4; ++r) dst[r] = cat8(lds_r64(ab + 64 * (xi + 1) + r * 1152), lds_r64(ab + 64 * (xi + 1) + r * 1152 + 8));
  };
  A[0] = loadA(0);
  loadB(0, B[0]);
#pragma unroll
  for (int st = 0; st < NS; ++st) {
    if (st + 1 < NS) {
      A[(st + 1) % WN] = loadA(st + 1);
      loadB(st + 1, B[(st + 1) & 1]);
    }
    if (WITH0 && st == NS - 3) { A0[0] = loadA(0); A0[1] = loadA(1); }
    SCHED();
#pragma unroll
    for (int t = 0; t < NT; ++t) {
      const int n = st - 2 * t;
      if (n >= 0) {
#pragma unroll
        for (int r = 0; r < 4; ++r) acc[t][r] = mfma16(A[n % WN], B[st & 1][r], acc[t][r]);
      }
    }
    if (WITH0 && st >= NS - 2) {
#pragma unroll
      for (int r = 0; r < 4; ++r) acc[NT][r] = mfma16(A0[st - (NS - 2)], B[st & 1][r], acc[NT][r]);
    }
    SCHED();
  }
#pragma unroll
  for (int t = 0; t < NTT; ++t) {
    const int ct = t < NT ? TOP - t : 0;
    const int blk = 64 * ct + 4 * a16 + g;
    const f32x4* z = acc[t];
    f32x4 y, c;
    y[0] = z[0][0];
    y[1] = z[0][1] + z[1][0];
    y[2] = z[0][2] + z[1][1] + z[2][0];
    y[3] = (z[0][3] + z[1][2]) + (z[2][1] + z[3][0]);
    c[0] = z[1][3] + z[2][2] + z[3][1];
    c[1] = z[2][3] + z[3][2];
    c[2] = z[3][3];
    c[3] = 0.f;
    lds_w128f(base + CH_Y + blk * 16, y);
    lds_w128f(base + CH_C + (blk + 1) * 16, c);
  }
}

template <int PH>
__global__ __launch_bounds__(512, 2) void kf_main(const u16* __restrict__ ssmk, const u16* __restrict__ ssmD,
                                                  char* __restrict__ ws, u16* __restrict__ out) {
  __shared__ __attribute__((aligned(16))) char smem[163840];
  const int tid = threadIdx.x, lane = tid & 63;
  const int w = __builtin_amdgcn_readfirstlane(tid >> 6);
  const int b = blockIdx.x, xcd = b & 7, rr = b >> 3;
  const unsigned sb = lds_addr(smem);
  const rsrc_t rs = __builtin_amdgcn_make_buffer_rsrc(ws, 0, WS_END, 0x00020000);
  const int g = lane >> 4, q = (lane >> 2) & 3, p = lane & 3, t16 = lane & 15;
  const int tb = 4 * (xcd & 3) + (rr & 3), nb = 8 * (xcd >> 2) + (rr >> 2);
  const bool isC = w < 4;
  const int hw = w & 3;
  STAMP(1, 0);

  u32x4 wf[W2][3];
  const unsigned woff = WS_WGU + (nb * 4 + hw) * 60 * 1024;

  if (PH & 1) {
    const bool conv = b < NCONV;
    const int nch = conv ? min(3, D - 3 * b) : 0;
    u32x4 xv[2], st4[4];
    float dval[2] = {0.f, 0.f};
    const int j = tid & 255;
    if (conv) {
      u32x4 pc[3];
#pragma unroll
      for (int i = 0; i < 3; ++i) {
        const int id = 3 * w + i, lc = id >> 3, sub = id & 7;
        const int ch = min(3 * b + lc, D - 1);
        if (sub < 4) pc[i] = ld_pl(rs, (lane >> 1) * 128 + (lane & 1) * 16, WS_UT + ch * 4096 + sub * 32);
        else pc[i] = *(const u32x4*)(ssmk + (size_t)ch * S + (sub - 4) * 512 + lane * 8);
      }
#pragma unroll
      for (int i = 0; i < 2; ++i) {
        const int ch = min(3 * b + (isC ? 0 : 1 + i), D - 1);
        xv[i] = ld_pl(rs, j * 16, WS_XT + ch * 4096);
        dval[i] = bf_lo(ssmD[ch]);
      }
#pragma unroll
      for (int i = 0; i < 4; ++i) st4[i] = ld_pl(rs, j * 64 + i * 16, WS_ST);
      if (w < 3) {
        const unsigned cb = sb + w * CH_BYTES;
        if (lane < 32) lds_w128(cb + CH_U + (lane >> 3) * 1152 + (lane & 7) * 16, (u32x4){0, 0, 0, 0});
        else if (lane < 34) lds_w128(cb + CH_K + (lane - 32) * 16, (u32x4){0, 0, 0, 0});
        else if (lane == 34) lds_w128(cb + CH_C, (u32x4){0, 0, 0, 0});
      }
#pragma unroll
      for (int i = 0; i < 3; ++i) {
        const int id = 3 * w + i, lc = id >> 3, sub = id & 7;
        const unsigned cb = sb + lc * CH_BYTES;
        lds_w128((sub < 4 ? cb + CH_U + sub * 1152 + 128 : cb + CH_K + 32 + (sub - 4) * 1024) + lane * 16, pc[i]);
      }
    }
    LGKM0_BAR();
    STAMP(1, 1);
    if (isC) {
      if (PH & 2) {
#pragma unroll
        for (int s = 0; s < W2; ++s)
#pragma unroll
          for (int jj = 0; jj < 3; ++jj) wf[s][jj] = ld_pl(rs, lane * 16, woff + (s * 3 + jj) * 1024);
        SCHED();
      }
      if (w < 3) {
        if (w < nch) conv_run<7, 4, true>(sb + w * CH_BYTES, lane);
      } else {
        for (int lc = 0; lc < nch; ++lc) conv_run<3, 3, false>(sb + lc * CH_BYTES, lane);
      }
    } else {
      if (b < 192) {
#pragma unroll
        for (int i = 0; i < 8; ++i) st_dev(rs, (tid - 256) * 16, WS_HH + b * 32768 + i * 4096, (u32x4){MARK, MARK, MARK, MARK});
        asm volatile("s_waitcnt vmcnt(0)" ::: "memory");
      }
    }
    STAMP(1, 2);
    LGKM0_BAR();
    STAMP(1, 3);
#pragma unroll
    for (int i = 0; i < 2; ++i) {
      const int lc = isC ? 0 : 1 + i;
      if ((i == 0 || !isC) && lc < nch) {
        const unsigned cb = sb + lc * CH_BYTES;
        const f32x4 y0 = lds_r128f(cb + CH_Y + j * 32) + lds_r128f(cb + CH_C + j * 32);
        const f32x4 y1 = lds_r128f(cb + CH_Y + j * 32 + 16) + lds_r128f(cb + CH_C + j * 32 + 16);
        unsigned uu[4];
#pragma unroll
        for (int r = 0; r < 4; ++r) uu[r] = *(LDS_AS const unsigned*)(uintptr_t)(cb + CH_U + r * 1152 + 128 + j * 4);
        float h[8];
#pragma unroll
        for (int e = 0; e < 8; ++e) {
          const float y = e < 4 ? y0[e & 3] : y1[e & 3];
          const float u = e < 4 ? bf_lo(uu[e & 3]) : bf_hi(uu[e & 3]);
          const float xx = (e & 1) ? bf_hi(xv[i][e >> 1]) : bf_lo(xv[i][e >> 1]);
          const float m = f_of(st4[e >> 1][2 * (e & 1)]), rsd = f_of(st4[e >> 1][2 * (e & 1) + 1]);
          h[e] = fmaf((xx - m) * rsd, fmaf(u, dval[i], y), xx);
        }
        u32x4 o = {unmark(pk_bf16(h[0], h[1])), unmark(pk_bf16(h[2], h[3])), unmark(pk_bf16(h[4], h[5])), unmark(pk_bf16(h[6], h[7]))};
        st_dev(rs, j * 16, WS_HT + (3 * b + lc) * 4096, o);
      }
    }
    STAMP(1, 4);
    if (PH & 2) LGKM0_BAR();
  }
  if (!(PH & 2)) return;

  const int half = nb & 1, db = nb >> 1;
  f32x4 acc[3][8];
  if (isC) {
    if (!(PH & 1)) {
#pragma unroll
      for (int s = 0; s < W2; ++s)
#pragma unroll
        for (int jj = 0; jj < 3; ++jj) wf[s][jj] = ld_pl(rs, lane * 16, woff + (s * 3 + jj) * 1024);
      SCHED();
    }
    unsigned ta[8];
    {
      const unsigned lb = sb + (4 * g + q) * 256 + 32 * (4 * (g & 1) + q) + 16 * (p >> 1) + 8 * (p & 1);
#pragma unroll
      for (int i = 0; i < 8; ++i) ta[i] = lb ^ (32 * i);
    }
#pragma unroll
    for (int jj = 0; jj < 3; ++jj)
#pragma unroll
      for (int i = 0; i < 8; ++i) acc[jj][i] = (f32x4){0, 0, 0, 0};
    bf16x8 af[2][4];
    auto loadH = [&](int hs, bf16x8 (&dst)[4]) {
      const int ks = hs >> 1;
      const unsigned o = (ks & 15) * 8192;
#pragma unroll
      for (int i = 0; i < 4; ++i) {
        const unsigned a = ta[4 * (hs & 1) + i] + (o & 0xffff) + (o >> 16) * 65536;
        dst[i] = cat8(tr_read(a), tr_read(a + 4096));
      }
    };
    RAW_BAR();
    STAMP(2, 1);
    loadH(0, af[0]);
#pragma unroll
    for (int hs = 0; hs < 40; ++hs) {
      const int ks = hs >> 1;
      if (hs + 1 < 40) {
        if (((hs + 1) & 3) == 0) RAW_BAR();
        loadH(hs + 1, af[(hs + 1) & 1]);
      }
      if ((hs & 1) == 0 && ks >= 1 && ks - 1 + W2 < 20) {
#pragma unroll
        for (int jj = 0; jj < 3; ++jj) wf[(ks - 1) % W2][jj] = ld_pl(rs, lane * 16, woff + ((ks - 1 + W2) * 3 + jj) * 1024);
      }
      SCHED();
#pragma unroll
      for (int i = 0; i < 4; ++i)
#pragma unroll
        for (int jj = 0; jj < 3; ++jj) acc[jj][4 * (hs & 1) + i] = mfma16(as_bf(wf[ks % W2][jj]), af[hs & 1][i], acc[jj][4 * (hs & 1) + i]);
      SCHED();
    }
    STAMP(2, 2);
  } else {
    if (lane < 48) {
      float c = 0.f;
#pragma unroll
      for (int kq = 0; kq < 5; ++kq) c += *(const float*)(ws + WS_CS + ((size_t)kq * 3072 + nb * 192 + hw * 48 + lane) * 4);
      *(float*)(smem + L_CS + (hw * 48 + lane) * 4) = c;
    }
    const int rsub = lane >> 2, c4 = lane & 3;
    const int r16 = (rsub & 12) | ((rsub & 1) << 1) | ((rsub >> 1) & 1);
    const unsigned goff = r16 * 4096 + 256 * tb + 64 * hw + 16 * c4;
    const unsigned loff = sb + r16 * 256 + (((4 * hw + c4) ^ (2 * (r16 & 7))) * 16);
    const unsigned roff = sb + L_RES + r16 * 128 + ((((hw & 1) * 4 + c4) ^ (((r16 >> 1) & 3) << 1)) * 16);
    u32x4 hv[40];
    if (PH & 1) asm volatile("s_waitcnt vmcnt(0)" ::: "memory");
    for (int sp = 0; sp < SPIN_CAP; ++sp) {
#pragma unroll
      for (int i = 0; i < 12; ++i) hv[i] = ld_dev(rs, goff, WS_HT + i * 65536);
      unsigned m = 0;
#pragma unroll
      for (int i = 0; i < 12; ++i) m = max(m, max4(hv[i]));
      if (__all(m != MARK)) break;
      __builtin_amdgcn_s_sleep(2);
    }
    SCHED();
#pragma unroll
    for (int i = 12; i < 40; ++i) hv[i] = ld_dev(rs, goff, WS_HT + i * 65536);
    SCHED();
    STAMP(2, 0);
    f32x2 sm[4], sq[4];
#pragma unroll
    for (int e = 0; e < 4; ++e) sm[e] = sq[e] = (f32x2){0.f, 0.f};
#pragma unroll
    for (int c = 0; c < 10; ++c) {
      if (c >= 3) {
        for (int sp = 0; sp < SPIN_CAP; ++sp) {
          unsigned m = 0;
#pragma unroll
          for (int i = 0; i < 4; ++i) m = max(m, max4(hv[4 * c + i]));
          if (__all(m != MARK)) break;
          __builtin_amdgcn_s_sleep(2);
#pragma unroll
          for (int i = 0; i < 4; ++i) hv[4 * c + i] = ld_dev(rs, goff, WS_HT + (4 * c + i) * 65536);
        }
      }
#pragma unroll
      for (int i = 0; i < 4; ++i) lds_w128(loff + (c & 7) * 16384 + i * 4096, hv[4 * c + i]);
      LGKM0_BAR();
#pragma unroll
      for (int i = 0; i < 4; ++i) {
        const int pi = 4 * c + i - 5 * db;
        if ((hw >> 1) == half && pi >= 0 && pi < 5) lds_w128(roff + pi * 2048, hv[4 * c + i]);
#pragma unroll
        for (int e = 0; e < 4; ++e) {
          const f32x2 v = {bf_lo(hv[4 * c + i][e]), bf_hi(hv[4 * c + i][e])};
          sm[e] += v;
          sq[e] += v * v;
        }
      }
      SCHED();
    }
    float v16[16];
#pragma unroll
    for (int e = 0; e < 4; ++e) { v16[2 * e] = sm[e][0]; v16[2 * e + 1] = sm[e][1]; v16[8 + 2 * e] = sq[e][0]; v16[9 + 2 * e] = sq[e][1]; }
    float v8[8], v4[4];
#pragma unroll
    for (int i = 0; i < 8; ++i) {
      auto r = __builtin_amdgcn_permlane32_swap(u_of(v16[i]), u_of(v16[i + 8]), false, false);
      v8[i] = f_of(r[0]) + f_of(r[1]);
    }
#pragma unroll
    for (int i = 0; i < 4; ++i) {
      auto r = __builtin_amdgcn_permlane16_swap(u_of(v8[i]), u_of(v8[i + 4]), false, false);
      float t = f_of(r[0]) + f_of(r[1]);
      t += dpp_f<0x124>(t);
      t += dpp_f<0x128>(t);
      v4[i] = t;
    }
    f32x4 o;
#pragma unroll
    for (int i = 0; i < 2; ++i) {
      auto r = __builtin_amdgcn_permlane32_swap(u_of(v4[i]), u_of(v4[i + 2]), false, false);
      const float mean = f_of(r[0]) * (1.0f / D);
      float var = fmaf(-mean, mean, f_of(r[1]) * (1.0f / D));
      var = rnd_bf16(fmaxf(var, 0.f));
      const float sd = rnd_bf16(sqrtf(rnd_bf16(var + EPS)));
      o[2 * i] = rnd_bf16(mean);
      o[2 * i + 1] = 1.0f / sd;
    }
    if (q == 0) lds_w128f(sb + L_ST + (32 * hw + 8 * c4 + 4 * (g & 1) + 2 * (g >> 1)) * 8, o);
    STAMP(2, 3);
  }
  LGKM0_BAR();
  STAMP(2, 4);

  const int kg = w & 3, th = w >> 2;
  const unsigned wdoff = WS_WD + (db * 48 + 12 * kg) * 5 * 1024;
  if (isC) {
    f32x4 cs[3];
#pragma unroll
    for (int jj = 0; jj < 3; ++jj) cs[jj] = lds_r128f(sb + L_CS + (w * 48 + jj * 16 + g * 4) * 4);
    const unsigned trb = sb + L_TR + w * 3072;
#pragma unroll
    for (int hf = 0; hf < 2; ++hf) {
#pragma unroll
      for (int i = 0; i < 4; ++i) {
        const int tt = 4 * hf + i;
        const f32x2 st = *(LDS_AS const f32x2*)(uintptr_t)(sb + L_ST + (tt * 16 + t16) * 8);
#pragma unroll
        for (int jj = 0; jj < 3; ++jj) {
          const f32x4 a = acc[jj][tt];
          const unsigned gp = pk_bf16((a[0] - st[0] * cs[jj][0]) * st[1], (a[1] - st[0] * cs[jj][1]) * st[1]);
          const unsigned up = pk_bf16((a[2] - st[0] * cs[jj][2]) * st[1], (a[3] - st[0] * cs[jj][3]) * st[1]);
          const float g0 = bf_lo(gp), g1 = bf_hi(gp);
          const unsigned o = pk_bf16((g0 > 0.f ? g0 : 0.f * g0) * bf_lo(up), (g1 > 0.f ? g1 : 0.f * g1) * bf_hi(up));
          *(LDS_AS unsigned*)(uintptr_t)(trb + i * 768 + jj * 256 + t16 * 16 + g * 4) = unmark(o);
        }
      }
      asm volatile("s_waitcnt lgkmcnt(0)" ::: "memory");
#pragma unroll
      for (int i = 0; i < 3; ++i) {
        const unsigned o = i * 1024 + lane * 16;
        const unsigned ti = o / 768, rem = o - ti * 768;
        const u32x4 v = lds_r128(trb + o);
        st_dev(rs, (tb * 8 + 4 * hf + ti) * 49152 + rem, WS_HH + (nb * 12 + w * 3) * 256, v);
      }
      CBAR();
    }
  } else {
#pragma unroll
    for (int i = 0; i < 30; ++i) glds16_asm(ws + wdoff + (25 + i) * 1024 + lane * 16, sb + hw * 30720 + i * 1024);
  }
  SCHED();
  u32x4 wr[5][5];
  if (isC) asm volatile("s_waitcnt vmcnt(0)" ::: "memory");
#pragma unroll
  for (int s = 0; s < 5; ++s)
#pragma unroll
    for (int jj = 0; jj < 5; ++jj) wr[s][jj] = ld_pl(rs, lane * 16, wdoff + (s * 5 + jj) * 1024);
  SCHED();
  STAMP(2, 5);
  RAW_BAR();
  if (!isC) {
#pragma unroll
    for (int i = 0; i < 5; ++i) glds16_asm(ws + wdoff + (55 + i) * 1024 + lane * 16, sb + L_TR + hw * 5120 + i * 1024);
  }

  {
    const unsigned hoff = WS_HH + ((tb * 8 + half * 4 + th * 2) * 192 + 48 * kg) * 256;
    u32x4 hf[3][4][2];
    auto loadG = [&](int gi) {
#pragma unroll
      for (int s = 0; s < 4; ++s)
#pragma unroll
        for (int i = 0; i < 2; ++i) hf[gi][s][i] = ld_dev(rs, lane * 16, hoff + i * 49152 + (4 * gi + s) * 1024);
    };
    auto checkG = [&](int gi) {
      for (int sp = 0; sp < SPIN_CAP; ++sp) {
        unsigned m = 0;
#pragma unroll
        for (int s = 0; s < 4; ++s)
#pragma unroll
          for (int i = 0; i < 2; ++i) m = max(m, max4(hf[gi][s][i]));
        if (__all(m != MARK)) break;
        __builtin_amdgcn_s_sleep(2);
        loadG(gi);
      }
    };
    loadG(0); loadG(1);
    SCHED();
    f32x4 a3[2][5];
#pragma unroll
    for (int i = 0; i < 2; ++i)
#pragma unroll
      for (int jj = 0; jj < 5; ++jj) a3[i][jj] = (f32x4){0, 0, 0, 0};
    checkG(0);
    STAMP(3, 0);
    RAW_BAR();
    const unsigned wl = sb + kg * 30720 + lane * 16;
    u32x4 wc[2][5];
#pragma unroll
    for (int s = 0; s < 12; ++s) {
      if (s == 4) checkG(1);
      if (s == 8) checkG(2);
      if (s + 1 >= 5 && s + 1 <= 10) {
#pragma unroll
        for (int jj = 0; jj < 5; ++jj) wc[(s + 1) & 1][jj] = lds_r128(wl + (s + 1 - 5) * 5120 + jj * 1024);
      }
      if (s == 11) {
        RAW_BAR();
#pragma unroll
        for (int jj = 0; jj < 5; ++jj) wc[1][jj] = lds_r128(sb + L_TR + kg * 5120 + jj * 1024 + lane * 16);
      }
      SCHED();
#pragma unroll
      for (int i = 0; i < 2; ++i)
#pragma unroll
        for (int jj = 0; jj < 5; ++jj)
          a3[i][jj] = mfma16(as_bf(s < 5 ? wr[s < 5 ? s : 0][jj] : wc[s & 1][jj]), as_bf(hf[s >> 2][s & 3][i]), a3[i][jj]);
      SCHED();
      if (s == 1) { loadG(2); SCHED(); }
    }
    STAMP(3, 1);
#pragma unroll
    for (int i = 0; i < 2; ++i)
#pragma unroll
      for (int jj = 0; jj < 5; ++jj) lds_w128f(sb + (kg * 20 + (th * 2 + i) * 5 + jj) * 1024 + lane * 16, a3[i][jj]);
    LGKM0_BAR();
    STAMP(3, 2);
#pragma unroll
    for (int r = 0; r < 3; ++r) {
      const int tl = w + 8 * r;
      if (tl < 20) {
        const int tt = tl / 5, dt = tl - 5 * tt;
        const unsigned a = sb + tl * 1024 + lane * 16;
        const f32x4 o = (lds_r128f(a) + lds_r128f(a + 20480)) + (lds_r128f(a + 40960) + lds_r128f(a + 61440));
        const int row = 16 * dt + 4 * g + q;
        const s16x4 hr = tr_read(sb + L_RES + row * 128 + ((tt ^ ((row >> 1) & 3)) * 32) + p * 8);
        const u32x2 hu = __builtin_bit_cast(u32x2, hr);
        const unsigned o01 = pk_bf16(o[0], o[1]), o23 = pk_bf16(o[2], o[3]);
        u32x2 ov = {pk_bf16(bf_lo(hu[0]) + bf_lo(o01), bf_hi(hu[0]) + bf_hi(o01)),
                    pk_bf16(bf_lo(hu[1]) + bf_lo(o23), bf_hi(hu[1]) + bf_hi(o23))};
        *(u32x2*)(out + (size_t)(128 * tb + 64 * half + 16 * tt + t16) * D + 80 * db + 16 * dt + 4 * g) = ov;
      }
    }
    STAMP(3, 3);
  }
}

extern "C" void kernel_launch(void* const* d_in, const int* in_sizes, int n_in, void* d_out, int out_size,
                              void* d_ws, size_t ws_size, hipStream_t stream) {
  const u16* q = (const u16*)d_in[0];
  const u16* k = (const u16*)d_in[1];
  const u16* v = (const u16*)d_in[2];
  const u16* shk = (const u16*)d_in[3];
  const u16* ssmk = (const u16*)d_in[4];
  const u16* ssmD = (const u16*)d_in[5];
  const u16* Wg = (const u16*)d_in[7];
  const u16* Wu = (const u16*)d_in[8];
  const u16* Wd = (const u16*)d_in[9];
  char* ws = (char*)d_ws;
  k0_prep<<<K0_TR + K0_ST + K0_GU + K0_DN, 256, 0, stream>>>(q, k, v, shk, Wg, Wu, Wd, ws);
  kf_main<3><<<256, 512, 0, stream>>>(ssmk, ssmD, ws, (u16*)d_out);
}
```

```cpp
#include <hip/hip_runtime.h>
#include <stdint.h>

typedef unsigned short u16;
typedef short s16x4 __attribute__((ext_vector_type(4)));
typedef short s16x8 __attribute__((ext_vector_type(8)));
typedef __bf16 bf16x8 __attribute__((ext_vector_type(8)));
typedef __bf16 bf16x2 __attribute__((ext_vector_type(2)));
typedef float f32x2 __attribute__((ext_vector_type(2)));
typedef float f32x4 __attribute__((ext_vector_type(4)));
typedef unsigned u32x2 __attribute__((ext_vector_type(2)));
typedef unsigned u32x4 __attribute__((ext_vector_type(4)));
#define LDS_AS __attribute__((address_space(3)))
#define DEVI __device__ __forceinline__

constexpr int S = 2048, D = 640, F = 1536;
constexpr float EPS = 1e-5f;
constexpr unsigned MARK = 0xffffffffu;
constexpr int SPIN_CAP = 1 << 14;

constexpr unsigned WS_UT = 0;
constexpr unsigned WS_XT = WS_UT + D * S * 2;
constexpr unsigned WS_HT = WS_XT + D * S * 2;
constexpr unsigned WS_ST = WS_HT + D * S * 2;
constexpr unsigned WS_CNT = WS_ST + S * 8;
constexpr unsigned WS_WD = WS_CNT + 2048;
constexpr unsigned WS_CS = WS_WD + D * F * 2;
constexpr unsigned WS_HH = WS_CS + 4 * 2 * F * 4;
constexpr unsigned WS_END = WS_HH + S * F * 2;

DEVI float bf_lo(unsigned u) { return __builtin_bit_cast(float, u << 16); }
DEVI float bf_hi(unsigned u) { return __builtin_bit_cast(float, u & 0xffff0000u); }
DEVI unsigned pk_bf16(float a, float b) {
  f32x2 v = {a, b};
  bf16x2 r = __builtin_convertvector(v, bf16x2);
  return __builtin_bit_cast(unsigned, r);
}
DEVI float rnd_bf16(float a) { return bf_lo(pk_bf16(a, a)); }
DEVI unsigned unmark(unsigned u) { return u < 0xfffffffeu ? u : 0xfffffffeu; }
DEVI s16x4 tr_read(unsigned lds_byte) {
  return __builtin_amdgcn_ds_read_tr16_b64_v4i16((LDS_AS s16x4*)(uintptr_t)lds_byte);
}
DEVI s16x4 lds_r64(unsigned a) { return *(volatile LDS_AS const s16x4*)(uintptr_t)a; }
DEVI u32x4 lds_r128(unsigned a) { return *(LDS_AS const u32x4*)(uintptr_t)a; }
DEVI void lds_w128(unsigned a, u32x4 v) { *(LDS_AS u32x4*)(uintptr_t)a = v; }
DEVI f32x4 lds_r128f(unsigned a) { return *(LDS_AS const f32x4*)(uintptr_t)a; }
DEVI void lds_w128f(unsigned a, f32x4 v) { *(LDS_AS f32x4*)(uintptr_t)a = v; }
DEVI bf16x8 cat8(s16x4 a, s16x4 b) {
  s16x8 ab = __builtin_shufflevector(a, b, 0, 1, 2, 3, 4, 5, 6, 7);
  return __builtin_bit_cast(bf16x8, ab);
}
DEVI bf16x8 as_bf(u32x4 v) { return __builtin_bit_cast(bf16x8, v); }
DEVI f32x4 mfma16(bf16x8 a, bf16x8 b, f32x4 c) { return __builtin_amdgcn_mfma_f32_16x16x32_bf16(a, b, c, 0, 0, 0); }
DEVI unsigned lds_addr(const void* p) { return (unsigned)(uintptr_t)(LDS_AS const char*)p; }
DEVI void glds16_asm(const void* gsrc, unsigned lds_dst) {
  unsigned keep;
  asm volatile("s_mov_b32 %0, m0\n\ts_mov_b32 m0, %2\n\ts_nop 0\n\tglobal_load_lds_dwordx4 %1, off sc1\n\ts_mov_b32 m0, %0"
               : "=&s"(keep) : "v"(gsrc), "s"(lds_dst) : "memory");
}
typedef __amdgpu_buffer_rsrc_t rsrc_t;
DEVI u32x4 ld_dev(rsrc_t rs, unsigned voff, unsigned soff) { return __builtin_amdgcn_raw_buffer_load_b128(rs, voff, soff, 16); }
DEVI u32x4 ld_pl(rsrc_t rs, unsigned voff, unsigned soff) { return __builtin_amdgcn_raw_buffer_load_b128(rs, voff, soff, 0); }
DEVI void st_dev(rsrc_t rs, unsigned voff, unsigned soff, u32x4 v) { __builtin_amdgcn_raw_buffer_store_b128(v, rs, voff, soff, 16); }
DEVI unsigned max4(u32x4 v) { return max(max(v[0], v[1]), max(v[2], v[3])); }
template <int CTRL> DEVI float dpp_f(float x) {
  return __builtin_bit_cast(float, __builtin_amdgcn_mov_dpp(__builtin_bit_cast(int, x), CTRL, 0xf, 0xf, true));
}
DEVI float f_of(unsigned u) { return __builtin_bit_cast(float, u); }
DEVI unsigned u_of(float f) { return __builtin_bit_cast(unsigned, f); }

#define STAMP(k, i) do {} while (0)
#define CBAR() asm volatile("" ::: "memory")
#define RAW_BAR() do { CBAR(); __builtin_amdgcn_s_barrier(); CBAR(); } while (0)
#define LGKM0_BAR() do { asm volatile("s_waitcnt lgkmcnt(0)" ::: "memory"); __builtin_amdgcn_s_barrier(); CBAR(); } while (0)
#define SCHED() __builtin_amdgcn_sched_barrier(0)

#define K0AUX 0
#define NOPOISON 0
#define K0GRID (K0_TR + K0_ST)
__global__ void k_tail(char* ws) { if (blockIdx.x == 9999) ws[0] = 1; }
constexpr int K0_TR = 320, K0_ST = 128;
__global__ __launch_bounds__(256) void k0_prep(const u16* __restrict__ x, const u16* __restrict__ kin,
                                               const u16* __restrict__ vin, const u16* __restrict__ shk,
                                               char* __restrict__ ws) {
  __shared__ __attribute__((aligned(16))) char smem[2 * 64 * 144];
  const int tid = threadIdx.x, lane = tid & 63;
  const int w = __builtin_amdgcn_readfirstlane(tid >> 6);
  const int b = blockIdx.x;
  const int g = lane >> 4, q = (lane >> 2) & 3, p = lane & 3;
  const rsrc_t rs0 = __builtin_amdgcn_make_buffer_rsrc(ws, 0, WS_END, 0x00020000);
#define K0ST(off, v) __builtin_amdgcn_raw_buffer_store_b128(v, rs0, off, 0, K0AUX)
  STAMP(0, 0);
  if (b < K0_TR) {
    const int tt = b & 31, cg = b >> 5;
    const int r2 = tid >> 3, cc = tid & 7;
    const int t0 = 64 * tt + 2 * r2, c0 = 64 * cg + 8 * cc;
    u32x4 kr[5], vr[2], xr[2], sw[4];
#pragma unroll
    for (int i = 0; i < 5; ++i) {
      const int t = t0 - 3 + i;
      const int tc = t < 0 ? 0 : t;
      kr[i] = *(const u32x4*)(kin + (size_t)tc * D + c0);
      if (t < 0) kr[i] = (u32x4){0, 0, 0, 0};
    }
#pragma unroll
    for (int i = 0; i < 2; ++i) {
      vr[i] = *(const u32x4*)(vin + (size_t)(t0 + i) * D + c0);
      xr[i] = *(const u32x4*)(x + (size_t)(t0 + i) * D + c0);
    }
#pragma unroll
    for (int i = 0; i < 4; ++i) sw[i] = *(const u32x4*)(shk + (size_t)c0 * 4 + 8 * i);
#pragma unroll
    for (int i = 0; i < 2; ++i) if (!NOPOISON) K0ST(WS_HT + b * 8192 + i * 4096 + tid * 16, ((u32x4){MARK, MARK, MARK, MARK}));
    char* su = smem;
    char* sx = smem + 64 * 144;
#pragma unroll
    for (int i = 0; i < 2; ++i) {
      u32x4 o;
#pragma unroll
      for (int cp = 0; cp < 4; ++cp) {
        float r[2];
#pragma unroll
        for (int hh = 0; hh < 2; ++hh) {
          const unsigned w01 = sw[cp][2 * hh], w23 = sw[cp][2 * hh + 1];
          float kk[4];
#pragma unroll
          for (int j = 0; j < 4; ++j) kk[j] = hh ? bf_hi(kr[i + 3 - j][cp]) : bf_lo(kr[i + 3 - j][cp]);
          float a = kk[3] * bf_hi(w23);
          a = fmaf(kk[2], bf_lo(w23), a);
          a = fmaf(kk[1], bf_hi(w01), a);
          a = fmaf(kk[0], bf_lo(w01), a);
          r[hh] = a;
        }
        const unsigned ks = pk_bf16(r[0], r[1]);
        o[cp] = pk_bf16(bf_lo(ks) * bf_lo(vr[i][cp]), bf_hi(ks) * bf_hi(vr[i][cp]));
      }
      *(u32x4*)(su + (2 * r2 + i) * 144 + cc * 16) = o;
      *(u32x4*)(sx + (2 * r2 + i) * 144 + cc * 16) = xr[i];
    }
    STAMP(0, 1);
    __syncthreads();
    STAMP(0, 2);
    const int c = 64 * cg + 16 * w + (lane & 15);
    const unsigned colb = (16 * w + 4 * p) * 2;
#pragma unroll
    for (int si = 0; si < 2; ++si) {
      const int ci = 4 * si + g;
      {
        const int rho = ci >> 1, half = ci & 1;
        const int row = 4 * (8 * half + q) + rho;
        const unsigned a = lds_addr(su) + row * 144 + colb;
        const bf16x8 v = cat8(tr_read(a), tr_read(a + 16 * 144));
        K0ST(WS_UT + ((c * 32 + tt) * 64 + ci * 8) * 2, __builtin_bit_cast(u32x4, v));
      }
      {
        const int row = 8 * ci + q;
        const unsigned a = lds_addr(sx) + row * 144 + colb;
        const bf16x8 v = cat8(tr_read(a), tr_read(a + 4 * 144));
        K0ST(WS_XT + (c * S + 64 * tt + ci * 8) * 2, __builtin_bit_cast(u32x4, v));
      }
    }
  } else {
    const int row = (b - K0_TR) * 16 + 4 * w + (lane >> 4), sub = lane & 15;
    if (b == K0_TR && tid < 8) *(unsigned*)(ws + WS_CNT + tid * 256) = 0u;
    u32x4 xr[5];
#pragma unroll
    for (int i = 0; i < 5; ++i) xr[i] = *(const u32x4*)(x + (size_t)row * D + (sub + 16 * i) * 8);
    float s = 0.f;
#pragma unroll
    for (int i = 0; i < 5; ++i)
#pragma unroll
      for (int j = 0; j < 4; ++j) s += bf_lo(xr[i][j]) + bf_hi(xr[i][j]);
    s += dpp_f<0xB1>(s); s += dpp_f<0x4E>(s); s += dpp_f<0x124>(s); s += dpp_f<0x128>(s);
    const float mean = s * (1.0f / D);
    float qq = 0.f;
#pragma unroll
    for (int i = 0; i < 5; ++i)
#pragma unroll
      for (int j = 0; j < 4; ++j) {
        const float a = bf_lo(xr[i][j]) - mean, c = bf_hi(xr[i][j]) - mean;
        qq = fmaf(a, a, qq);
        qq = fmaf(c, c, qq);
      }
    qq += dpp_f<0xB1>(qq); qq += dpp_f<0x4E>(qq); qq += dpp_f<0x124>(qq); qq += dpp_f<0x128>(qq);
    const float var = rnd_bf16(qq * (1.0f / D));
    const float sd = rnd_bf16(sqrtf(rnd_bf16(var + EPS)));
    if (sub == 0) {
      f32x2 st = {rnd_bf16(mean), 1.0f / sd};
      *(f32x2*)(ws + WS_ST + (size_t)row * 8) = st;
    }
  }
  STAMP(0, 3);
}

constexpr int NCONV = 214;
constexpr int CH_U = 0, CH_K = 4608, CH_Y = CH_K + 4160, CH_C = CH_Y + 8192, CH_BYTES = CH_C + 8224;
constexpr int L_WA7 = 49152, L_RES = 61440, L_ST = L_RES + 10240, L_CS = L_ST + 1024, L_CNT = 76000, L_WA = 76800;
constexpr int L_SP = 16384;
constexpr int WREG = 6;

template <int TOP, int NT, bool WITH0>
DEVI void conv_run(unsigned base, int lane) {
  const int a16 = lane & 15, g = lane >> 4, q = (lane >> 2) & 3, pp = lane & 3;
  const unsigned ab = base + CH_U + 128 + 2 * (4 * a16 - 4 - 8 * g) - 64;
  const unsigned aa = base + CH_K + 32 + 2 * (32 * g - 4 * q + 4 * pp);
  constexpr int NTT = NT + (WITH0 ? 1 : 0);
  constexpr int NS = 2 * TOP + 2;
  constexpr int WN = 2 * NT;
  f32x4 acc[NTT][4];
#pragma unroll
  for (int t = 0; t < NTT; ++t)
#pragma unroll
    for (int r = 0; r < 4; ++r) acc[t][r] = (f32x4){0, 0, 0, 0};
  bf16x8 A[WN], B[2][4], A0[2];
  auto loadA = [&](int n) { return cat8(tr_read(aa + 256 * n + 32), tr_read(aa + 256 * n)); };
  auto loadB = [&](int st, bf16x8 (&dst)[4]) {
    const int xi = 2 * TOP - st;
#pragma unroll
    for (int r = 0; r < 4; ++r) dst[r] = cat8(lds_r64(ab + 64 * (xi + 1) + r * 1152), lds_r64(ab + 64 * (xi + 1) + r * 1152 + 8));
  };
  A[0] = loadA(0);
  loadB(0, B[0]);
#pragma unroll
  for (int st = 0; st < NS; ++st) {
    if (st + 1 < NS) {
      A[(st + 1) % WN] = loadA(st + 1);
      loadB(st + 1, B[(st + 1) & 1]);
    }
    if (WITH0 && st == NS - 3) { A0[0] = loadA(0); A0[1] = loadA(1); }
    SCHED();
#pragma unroll
    for (int t = 0; t < NT; ++t) {
      const int n = st - 2 * t;
      if (n >= 0) {
#pragma unroll
        for (int r = 0; r < 4; ++r) acc[t][r] = mfma16(A[n % WN], B[st & 1][r], acc[t][r]);
      }
    }
    if (WITH0 && st >= NS - 2) {
#pragma unroll
      for (int r = 0; r < 4; ++r) acc[NT][r] = mfma16(A0[st - (NS - 2)], B[st & 1][r], acc[NT][r]);
    }
    SCHED();
  }
#pragma unroll
  for (int t = 0; t < NTT; ++t) {
    const int ct = t < NT ? TOP - t : 0;
    const int blk = 64 * ct + 4 * a16 + g;
    const f32x4* z = acc[t];
    f32x4 y, c;
    y[0] = z[0][0];
    y[1] = z[0][1] + z[1][0];
    y[2] = z[0][2] + z[1][1] + z[2][0];
    y[3] = (z[0][3] + z[1][2]) + (z[2][1] + z[3][0]);
    c[0] = z[1][3] + z[2][2] + z[3][1];
    c[1] = z[2][3] + z[3][2];
    c[2] = z[3][3];
    c[3] = 0.f;
    lds_w128f(base + CH_Y + blk * 16, y);
    lds_w128f(base + CH_C + (blk + 1) * 16, c);
  }
}

template <int PH>
__global__ __launch_bounds__(512, 2) void kf_main(const u16* __restrict__ ssmk, const u16* __restrict__ ssmD,
                                                  const u16* __restrict__ Wg, const u16* __restrict__ Wu,
                                                  const u16* __restrict__ Wd, char* __restrict__ ws,
                                                  u16* __restrict__ out) {
  __shared__ __attribute__((aligned(16))) char smem[163840];
  const int tid = threadIdx.x, lane = tid & 63;
  const int w = __builtin_amdgcn_readfirstlane(tid >> 6);
  const int b = blockIdx.x, xcd = b & 7, rr = b >> 3;
  const unsigned sb = lds_addr(smem);
  const rsrc_t rs = __builtin_amdgcn_make_buffer_rsrc(ws, 0, WS_END, 0x00020000);
  const int g = lane >> 4, q = (lane >> 2) & 3, p = lane & 3, t16 = lane & 15;
  const int tb = 4 * (xcd & 3) + (rr & 3), nb = 8 * (xcd >> 2) + (rr >> 2);
  const int half = nb & 1, db = nb >> 1;
  const bool isC = w < 4;
  const int hw = w & 3;
  const bool conv = b < NCONV;
  const int nch = conv ? min(3, D - 3 * b) : 0;
  LDS_AS unsigned* cnt = (LDS_AS unsigned*)(uintptr_t)(sb + L_CNT);
  STAMP(1, 0);
  if (tid == 0) { cnt[0] = 0; cnt[1] = 0; cnt[2] = 0; }

  if (!conv) {
    const int u = (b - NCONV) * 8 + w;
    if (u < 240) {
      const int ks2 = u / 5, dq = u % 5;
      constexpr int RS = 288;
      const unsigned sm = sb + w * 32 * RS;
      u32x4 v8[8];
#pragma unroll
      for (int i = 0; i < 8; ++i) v8[i] = *(const u32x4*)(Wd + (size_t)(32 * ks2 + 4 * i + g) * D + 128 * dq + 8 * t16);
#pragma unroll
      for (int i = 0; i < 8; ++i) lds_w128(sm + (4 * i + g) * RS + t16 * 16, v8[i]);
      asm volatile("s_waitcnt lgkmcnt(0)" ::: "memory");
#pragma unroll
      for (int t = 0; t < 8; ++t) {
        const int dt = 8 * dq + t;
        const unsigned a = sm + (8 * g + q) * RS + t * 32 + p * 8;
        const bf16x8 v = cat8(tr_read(a), tr_read(a + 4 * RS));
        st_dev(rs, lane * 16, WS_WD + (((dt / 5) * 48 + ks2) * 5 + (dt % 5)) * 1024, __builtin_bit_cast(u32x4, v));
      }
    } else {
      const int cu = u - 240, fb = cu >> 2, kq = cu & 3;
      f32x2 sa[2][4];
#pragma unroll
      for (int m = 0; m < 2; ++m)
#pragma unroll
        for (int e = 0; e < 4; ++e) sa[m][e] = (f32x2){0.f, 0.f};
#pragma unroll
      for (int m = 0; m < 2; ++m) {
        const u16* src = (m ? Wu : Wg) + (size_t)(160 * kq + (lane >> 3)) * F + 64 * fb + 8 * (lane & 7);
        u32x4 v[20];
#pragma unroll
        for (int i = 0; i < 20; ++i) v[i] = *(const u32x4*)(src + (size_t)8 * i * F);
#pragma unroll
        for (int i = 0; i < 20; ++i)
#pragma unroll
          for (int e = 0; e < 4; ++e) sa[m][e] += (f32x2){bf_lo(v[i][e]), bf_hi(v[i][e])};
      }
      f32x4 o[4];
#pragma unroll
      for (int e = 0; e < 4; ++e) o[e] = (f32x4){sa[0][e][0], sa[0][e][1], sa[1][e][0], sa[1][e][1]};
      f32x4 r2[2], r1;
#pragma unroll
      for (int i = 0; i < 2; ++i)
#pragma unroll
        for (int c = 0; c < 4; ++c) {
          auto r = __builtin_amdgcn_permlane32_swap(u_of(o[i][c]), u_of(o[i + 2][c]), false, false);
          r2[i][c] = f_of(r[0]) + f_of(r[1]);
        }
#pragma unroll
      for (int c = 0; c < 4; ++c) {
        auto r = __builtin_amdgcn_permlane16_swap(u_of(r2[0][c]), u_of(r2[1][c]), false, false);
        float t = f_of(r[0]) + f_of(r[1]);
        r1[c] = t + dpp_f<0x128>(t);
      }
      const unsigned off = (lane & 8) ? 0x80000000u : (unsigned)((lane & 7) * 64 + (2 * (lane >> 5) + ((lane >> 4) & 1)) * 16);
      st_dev(rs, off, WS_CS + (kq * 192 + fb * 8) * 64, __builtin_bit_cast(u32x4, r1));
    }
    asm volatile("s_waitcnt vmcnt(0)" ::: "memory");
    __syncthreads();
    if (tid < 8) __hip_atomic_fetch_add((__attribute__((address_space(1))) unsigned*)(ws + WS_CNT + tid * 256), 1u, __ATOMIC_RELAXED, __HIP_MEMORY_SCOPE_AGENT);
  }

  f32x4 acc[3][8];
  const int kg = w & 3, th = w >> 2;
  const unsigned wdoff = WS_WD + (db * 48 + 12 * kg) * 5 * 1024;
  u32x4 wh[5][5];

  if (isC) {
    u32x4 xv[3], st4[4];
    float dval[3];
    if (conv) {
#pragma unroll
      for (int i = 0; i < 3; ++i) {
        const int ch = min(3 * b + i, D - 1);
        xv[i] = ld_pl(rs, tid * 16, WS_XT + ch * 4096);
        dval[i] = bf_lo(ssmD[ch]);
      }
#pragma unroll
      for (int i = 0; i < 4; ++i) st4[i] = ld_pl(rs, tid * 64 + i * 16, WS_ST);
      if (w < 3) {
        const unsigned cb = sb + w * CH_BYTES;
        if (lane < 32) lds_w128(cb + CH_U + (lane >> 3) * 1152 + (lane & 7) * 16, (u32x4){0, 0, 0, 0});
        else if (lane < 34) lds_w128(cb + CH_K + (lane - 32) * 16, (u32x4){0, 0, 0, 0});
        else if (lane == 34) lds_w128(cb + CH_C, (u32x4){0, 0, 0, 0});
      }
    }
    LGKM0_BAR();
    STAMP(1, 1);
    if (conv) {
      if (w < 3) {
        if (w < nch) conv_run<7, 4, true>(sb + w * CH_BYTES, lane);
      } else {
        for (int lc = 0; lc < nch; ++lc) conv_run<3, 3, false>(sb + lc * CH_BYTES, lane);
      }
      STAMP(1, 2);
      asm volatile("s_waitcnt lgkmcnt(0)" ::: "memory");
      if (lane == 0) __hip_atomic_fetch_add(cnt, 1u, __ATOMIC_RELAXED, __HIP_MEMORY_SCOPE_WORKGROUP);
      for (int sp = 0; sp < (1 << 20); ++sp) {
        if (__builtin_amdgcn_readfirstlane(*(volatile LDS_AS unsigned*)cnt) >= 8u) break;
        __builtin_amdgcn_s_sleep(1);
      }
      CBAR();
      STAMP(1, 3);
#pragma unroll
      for (int lc = 0; lc < 3; ++lc) {
        if (lc < nch) {
          const unsigned cb = sb + lc * CH_BYTES;
          const f32x4 y0 = lds_r128f(cb + CH_Y + tid * 32) + lds_r128f(cb + CH_C + tid * 32);
          const f32x4 y1 = lds_r128f(cb + CH_Y + tid * 32 + 16) + lds_r128f(cb + CH_C + tid * 32 + 16);
          unsigned uu[4];
#pragma unroll
          for (int r = 0; r < 4; ++r) uu[r] = *(LDS_AS const unsigned*)(uintptr_t)(cb + CH_U + r * 1152 + 128 + tid * 4);
          float h[8];
#pragma unroll
          for (int e = 0; e < 8; ++e) {
            const float y = e < 4 ? y0[e & 3] : y1[e & 3];
            const float u = e < 4 ? bf_lo(uu[e & 3]) : bf_hi(uu[e & 3]);
            const float xx = (e & 1) ? bf_hi(xv[lc][e >> 1]) : bf_lo(xv[lc][e >> 1]);
            const float m = f_of(st4[e >> 1][2 * (e & 1)]), rsd = f_of(st4[e >> 1][2 * (e & 1) + 1]);
            h[e] = fmaf((xx - m) * rsd, fmaf(u, dval[lc], y), xx);
          }
          u32x4 o = {unmark(pk_bf16(h[0], h[1])), unmark(pk_bf16(h[2], h[3])), unmark(pk_bf16(h[4], h[5])), unmark(pk_bf16(h[6], h[7]))};
          st_dev(rs, tid * 16, WS_HT + (3 * b + lc) * 4096, o);
        }
      }
    }
    STAMP(1, 4);
    LGKM0_BAR();
    unsigned wa[3];
    {
      const int r = 4 * g + q;
#pragma unroll
      for (int jj = 0; jj < 3; ++jj) wa[jj] = sb + (r >> 1) * 768 + ((12 * (r & 1) + 3 * w + jj + ((r >> 1) & 3)) % 24) * 32 + p * 8;
    }
    auto loadW = [&](int slot, bf16x8 (&dst)[3]) {
      const unsigned o = slot < 7 ? L_WA + slot * 12288 : L_WA7;
#pragma unroll
      for (int jj = 0; jj < 3; ++jj) dst[jj] = cat8(tr_read(wa[jj] + o), tr_read(wa[jj] + o + 6144));
    };
    bf16x8 wr[WREG][3], wl[2][3];
#pragma unroll
    for (int s = 0; s < WREG; ++s) loadW(s, wr[s]);
    LGKM0_BAR();
    unsigned ta[8];
    {
      const unsigned lb = sb + (4 * g + q) * 256 + 32 * (4 * (g & 1) + q) + 16 * (p >> 1) + 8 * (p & 1);
#pragma unroll
      for (int i = 0; i < 8; ++i) ta[i] = lb ^ (32 * i);
    }
#pragma unroll
    for (int jj = 0; jj < 3; ++jj)
#pragma unroll
      for (int i = 0; i < 8; ++i) acc[jj][i] = (f32x4){0, 0, 0, 0};
    bf16x8 af[2][4];
    auto loadH = [&](int hs, bf16x8 (&dst)[4]) {
      const int ks = hs >> 1;
      const unsigned o = ((ks >> 1) % 3) * 16384 + (ks & 1) * 8192;
#pragma unroll
      for (int i = 0; i < 4; ++i) dst[i] = cat8(tr_read(ta[4 * (hs & 1) + i] + o), tr_read(ta[4 * (hs & 1) + i] + o + 4096));
    };
    RAW_BAR();
    STAMP(2, 1);
    loadH(0, af[0]);
#pragma unroll
    for (int hs = 0; hs < 40; ++hs) {
      const int ks = hs >> 1;
      if (hs + 1 < 40) {
        if (((hs + 1) & 3) == 0) RAW_BAR();
        loadH(hs + 1, af[(hs + 1) & 1]);
        if ((hs & 1) && ks + 1 >= WREG) loadW((ks + 1) & 7, wl[(ks + 1) & 1]);
      }
      SCHED();
#pragma unroll
      for (int i = 0; i < 4; ++i)
#pragma unroll
        for (int jj = 0; jj < 3; ++jj)
          acc[jj][4 * (hs & 1) + i] = mfma16(ks < WREG ? wr[ks < WREG ? ks : 0][jj] : wl[ks & 1][jj], af[hs & 1][i], acc[jj][4 * (hs & 1) + i]);
      SCHED();
    }
    STAMP(2, 2);
    LGKM0_BAR();
    STAMP(2, 4);
    f32x4 cs[3];
#pragma unroll
    for (int jj = 0; jj < 3; ++jj) cs[jj] = lds_r128f(sb + L_CS + (w * 48 + jj * 16 + g * 4) * 4);
    const unsigned trb = sb + w * 3072;
#pragma unroll
    for (int hf = 0; hf < 2; ++hf) {
#pragma unroll
      for (int i = 0; i < 4; ++i) {
        const int tt = 4 * hf + i;
        const f32x2 st = *(LDS_AS const f32x2*)(uintptr_t)(sb + L_ST + (tt * 16 + t16) * 8);
#pragma unroll
        for (int jj = 0; jj < 3; ++jj) {
          const f32x4 a = acc[jj][tt];
          const unsigned gp = pk_bf16((a[0] - st[0] * cs[jj][0]) * st[1], (a[1] - st[0] * cs[jj][1]) * st[1]);
          const unsigned up = pk_bf16((a[2] - st[0] * cs[jj][2]) * st[1], (a[3] - st[0] * cs[jj][3]) * st[1]);
          const float g0 = bf_lo(gp), g1 = bf_hi(gp);
          const unsigned o = pk_bf16((g0 > 0.f ? g0 : 0.f * g0) * bf_lo(up), (g1 > 0.f ? g1 : 0.f * g1) * bf_hi(up));
          *(LDS_AS unsigned*)(uintptr_t)(trb + i * 768 + jj * 256 + t16 * 16 + g * 4) = unmark(o);
        }
      }
      asm volatile("s_waitcnt lgkmcnt(0)" ::: "memory");
#pragma unroll
      for (int i = 0; i < 3; ++i) {
        const unsigned o = i * 1024 + lane * 16;
        const unsigned ti = o / 768, rem = o - ti * 768;
        const u32x4 v = lds_r128(trb + o);
        st_dev(rs, (tb * 8 + 4 * hf + ti) * 49152 + rem, WS_HH + (nb * 12 + w * 3) * 256, v);
      }
      CBAR();
    }
    STAMP(2, 5);
    asm volatile("s_waitcnt vmcnt(0)" ::: "memory");
  } else {
    const rsrc_t rg = __builtin_amdgcn_make_buffer_rsrc((void*)Wg, 0, D * F * 2, 0x00020000);
    const rsrc_t ru = __builtin_amdgcn_make_buffer_rsrc((void*)Wu, 0, D * F * 2, 0x00020000);
    if (conv) {
      u32x4 pc[6];
#pragma unroll
      for (int i = 0; i < 6; ++i) {
        const int id = 6 * hw + i, lc = id >> 3, sub = id & 7;
        const int ch = min(3 * b + lc, D - 1);
        if (sub < 4) pc[i] = ld_pl(rs, lane * 16, WS_UT + ch * 4096 + sub * 1024);
        else pc[i] = *(const u32x4*)(ssmk + (size_t)ch * S + (sub - 4) * 512 + lane * 8);
      }
      const unsigned uo = ((lane >> 1) & 3) * 1152 + 128 + ((lane >> 3) * 2 + (lane & 1)) * 16;
#pragma unroll
      for (int i = 0; i < 6; ++i) {
        const int id = 6 * hw + i, lc = id >> 3, sub = id & 7;
        const unsigned cb = sb + lc * CH_BYTES;
        lds_w128(sub < 4 ? cb + CH_U + uo + sub * 256 : cb + CH_K + 32 + (sub - 4) * 1024 + lane * 16, pc[i]);
      }
    }
    LGKM0_BAR();
    STAMP(1, 1);
    if (b < 192) {
#pragma unroll
      for (int i = 0; i < 8; ++i) st_dev(rs, (tid - 256) * 16, WS_HH + b * 32768 + i * 4096, (u32x4){MARK, MARK, MARK, MARK});
    }
    const int wrw = lane / 12, wc = lane - 12 * wrw;
    const unsigned wgo = lane < 48 ? (unsigned)(((8 * hw + wrw) * F + 96 * nb + 8 * wc) * 2) : 0x80000000u;
    unsigned wlo[2];
#pragma unroll
    for (int i = 0; i < 2; ++i) {
      const int r = 8 * hw + 4 * i + wrw;
      wlo[i] = sb + (r >> 1) * 768 + ((12 * (r & 1) + wc + ((r >> 1) & 3)) % 24) * 32;
    }
    u32x4 W[7][4];
    auto Wld = [&](int ks, u32x4 (&d)[4]) {
#pragma unroll
      for (int i = 0; i < 2; ++i) {
        d[2 * i] = ld_pl(rg, wgo, (32 * ks + 4 * i) * F * 2);
        d[2 * i + 1] = ld_pl(ru, wgo, (32 * ks + 4 * i) * F * 2);
      }
      SCHED();
    };
    auto Wst = [&](int ks, u32x4 (&d)[4]) {
      const int slot = ks & 7;
      const unsigned o = slot < 7 ? L_WA + slot * 12288 : L_WA7;
      if (lane < 48) {
#pragma unroll
        for (int i = 0; i < 2; ++i) {
          lds_w128(wlo[i] + o, (u32x4){d[2 * i][0], d[2 * i + 1][0], d[2 * i][1], d[2 * i + 1][1]});
          lds_w128(wlo[i] + o + 16, (u32x4){d[2 * i][2], d[2 * i + 1][2], d[2 * i][3], d[2 * i + 1][3]});
        }
      }
      SCHED();
    };
#pragma unroll
    for (int s = 0; s < 4; ++s) Wld(s, W[s]);
    asm volatile("s_waitcnt vmcnt(16)" ::: "memory");
    if (lane == 0) __hip_atomic_fetch_add(cnt, 1u, __ATOMIC_RELAXED, __HIP_MEMORY_SCOPE_WORKGROUP);
#pragma unroll
    for (int s = 4; s < 7; ++s) Wld(s, W[s]);
#pragma unroll
    for (int s = 0; s < 7; ++s) { Wst(s, W[s]); Wld(s + 7, W[s]); }
    STAMP(1, 4);
    LGKM0_BAR();
    Wst(7, W[0]);
    RAW_BAR();
    const unsigned goff = g * 4096 + 256 * tb + 16 * t16;
    u32x4 hv[4][4];
    auto Hld = [&](int c, u32x4 (&d)[4]) {
#pragma unroll
      for (int i = 0; i < 4; ++i) d[i] = ld_dev(rs, goff, WS_HT + (64 * c + 16 * hw + 4 * i) * 4096);
      SCHED();
    };
    Hld(0, hv[0]); Hld(1, hv[1]);
#pragma unroll
    for (int s = 8; s < 14; ++s) Wst(s, W[s - 7]);
    for (int sp = 0; sp < SPIN_CAP; ++sp) {
      unsigned m = 0;
#pragma unroll
      for (int i = 0; i < 8; ++i) m = max(m, max4(hv[i >> 2][i & 3]));
      if (__all(m != MARK)) break;
      __builtin_amdgcn_s_sleep(2);
      Hld(0, hv[0]); Hld(1, hv[1]);
    }
    Hld(2, hv[2]); Hld(3, hv[3]);
    STAMP(2, 0);
    const unsigned lo0 = sb + (16 * hw + g) * 256 + ((t16 ^ (2 * g)) * 16);
    const unsigned lo1 = sb + (16 * hw + 4 + g) * 256 + ((t16 ^ (2 * (4 + g))) * 16);
    f32x2 sm[4], sq[4];
#pragma unroll
    for (int e = 0; e < 4; ++e) sm[e] = sq[e] = (f32x2){0.f, 0.f};
    float csum = 0.f;
#pragma unroll
    for (int c = 0; c < 10; ++c) {
      u32x4(&hc)[4] = hv[c & 3];
      if (c >= 2) {
        for (int sp = 0; sp < SPIN_CAP; ++sp) {
          unsigned m = 0;
#pragma unroll
          for (int i = 0; i < 4; ++i) m = max(m, max4(hc[i]));
          if (__all(m != MARK)) break;
          __builtin_amdgcn_s_sleep(2);
          Hld(c, hc);
        }
      }
#pragma unroll
      for (int i = 0; i < 4; ++i) lds_w128(((i & 1) ? lo1 : lo0) + (c % 3) * 16384 + (i >> 1) * 2048, hc[i]);
      if (c >= 6 && c <= 8) { Wst(2 * c + 2, W[2 * (c - 6)]); Wst(2 * c + 3, W[2 * (c - 6) + 1]); }
      if (c == 9 && lane < 48) *(LDS_AS float*)(uintptr_t)(sb + L_CS + (hw * 48 + lane) * 4) = csum;
      LGKM0_BAR();
      {
        const int pi = 4 * c + hw - 5 * db;
        if (pi >= 0 && pi < 5 && (t16 >> 3) == half) {
#pragma unroll
          for (int i = 0; i < 4; ++i) {
            const int row = 4 * i + g;
            lds_w128(sb + L_RES + (16 * pi + row) * 128 + (((t16 & 7) ^ (((row >> 1) & 3) << 1)) * 16), hc[i]);
          }
        }
      }
#pragma unroll
      for (int i = 0; i < 4; ++i)
#pragma unroll
        for (int e = 0; e < 4; ++e) {
          const f32x2 v = {bf_lo(hc[i][e]), bf_hi(hc[i][e])};
          sm[e] += v;
          sq[e] += v * v;
        }
#pragma unroll
      for (int e = 0; e < 4; ++e) asm volatile("" : "+v"(sm[e]), "+v"(sq[e]));
      SCHED();
      if (c + 4 < 10) Hld(c + 4, hc);
      if (c < 3) { Wld(14 + 2 * c, W[2 * c]); Wld(15 + 2 * c, W[2 * c + 1]); }
      if (c == 3) {
        for (int sp = 0; sp < SPIN_CAP; ++sp) {
          const unsigned v = __builtin_amdgcn_raw_buffer_load_b32(rs, 0, WS_CNT + xcd * 256, 16);
          if (__builtin_amdgcn_readfirstlane(v) >= 256 - NCONV) break;
          __builtin_amdgcn_s_sleep(2);
        }
        CBAR();
#pragma unroll
        for (int kq = 0; kq < 4; ++kq)
          csum += f_of(__builtin_amdgcn_raw_buffer_load_b32(rs, lane < 48 ? lane * 4 : 0x80000000u, WS_CS + (kq * 3072 + nb * 192 + hw * 48) * 4, 16));
      }
      if (c >= 6 && c <= 8) {
#pragma unroll
        for (int t = 2 * (c - 6); t < 2 * (c - 6) + 2 && t < 5; ++t)
#pragma unroll
          for (int jj = 0; jj < 5; ++jj) wh[t][jj] = ld_dev(rs, lane * 16, wdoff + ((7 + t) * 5 + jj) * 1024);
        SCHED();
      }
    }
    {
      float v16[16], v8[8];
#pragma unroll
      for (int e = 0; e < 4; ++e) { v16[2 * e] = sm[e][0]; v16[2 * e + 1] = sm[e][1]; v16[8 + 2 * e] = sq[e][0]; v16[9 + 2 * e] = sq[e][1]; }
#pragma unroll
      for (int i = 0; i < 8; ++i) {
        auto r = __builtin_amdgcn_permlane32_swap(u_of(v16[i]), u_of(v16[i + 8]), false, false);
        v8[i] = f_of(r[0]) + f_of(r[1]);
      }
      f32x4 v4;
#pragma unroll
      for (int i = 0; i < 4; ++i) {
        auto r = __builtin_amdgcn_permlane16_swap(u_of(v8[i]), u_of(v8[i + 4]), false, false);
        v4[i] = f_of(r[0]) + f_of(r[1]);
      }
      lds_w128f(sb + L_SP + hw * 1024 + (g >> 1) * 512 + (8 * t16 + 4 * (g & 1)) * 4, v4);
      asm volatile("s_waitcnt lgkmcnt(0)" ::: "memory");
      unsigned old = 0;
      if (lane == 0) old = __hip_atomic_fetch_add(cnt + 1, 1u, __ATOMIC_RELAXED, __HIP_MEMORY_SCOPE_WORKGROUP);
      if (__builtin_amdgcn_readfirstlane(old) == 3u) {
        CBAR();
        f32x2 s2 = {0.f, 0.f}, q2 = {0.f, 0.f};
#pragma unroll
        for (int i = 0; i < 4; ++i) {
          s2 += *(LDS_AS const f32x2*)(uintptr_t)(sb + L_SP + i * 1024 + lane * 8);
          q2 += *(LDS_AS const f32x2*)(uintptr_t)(sb + L_SP + i * 1024 + 512 + lane * 8);
        }
        f32x4 o;
#pragma unroll
        for (int i = 0; i < 2; ++i) {
          const float mean = s2[i] * (1.0f / D);
          float var = fmaf(-mean, mean, q2[i] * (1.0f / D));
          var = rnd_bf16(fmaxf(var, 0.f));
          const float sd = rnd_bf16(sqrtf(rnd_bf16(var + EPS)));
          o[2 * i] = rnd_bf16(mean);
          o[2 * i + 1] = 1.0f / sd;
        }
        lds_w128f(sb + L_ST + lane * 16, o);
      }
    }
    STAMP(2, 3);
    LGKM0_BAR();
#pragma unroll
    for (int i = 0; i < 20; ++i) glds16_asm(ws + wdoff + i * 1024 + lane * 16, sb + L_WA + (i / 5) * 20480 + hw * 5120 + (i % 5) * 1024);
#pragma unroll
    for (int i = 25; i < 35; ++i) glds16_asm(ws + wdoff + i * 1024 + lane * 16, sb + (i / 5 - 4) * 20480 + hw * 5120 + (i % 5) * 1024);
    STAMP(2, 5);
  }
  RAW_BAR();

  {
    const unsigned hoff = WS_HH + ((tb * 8 + half * 4 + th * 2) * 192 + 48 * kg) * 256;
    u32x4 hf[2][4][2];
    auto loadG = [&](int gi) {
#pragma unroll
      for (int s = 0; s < 4; ++s)
#pragma unroll
        for (int i = 0; i < 2; ++i) hf[gi & 1][s][i] = ld_dev(rs, lane * 16, hoff + i * 49152 + (4 * gi + s) * 1024);
    };
    auto checkG = [&](int gi) {
      for (int sp = 0; sp < SPIN_CAP; ++sp) {
        unsigned m = 0;
#pragma unroll
        for (int s = 0; s < 4; ++s)
#pragma unroll
          for (int i = 0; i < 2; ++i) m = max(m, max4(hf[gi & 1][s][i]));
        if (__all(m != MARK)) break;
        __builtin_amdgcn_s_sleep(2);
        loadG(gi);
      }
    };
    loadG(0);
    SCHED();
    if (!isC) {
#pragma unroll
      for (int i = 0; i < 5; ++i) glds16_asm(ws + wdoff + (20 + i) * 1024 + lane * 16, sb + hw * 5120 + i * 1024);
    }
    loadG(1);
    SCHED();
    f32x4 a3[2][5];
#pragma unroll
    for (int i = 0; i < 2; ++i)
#pragma unroll
      for (int jj = 0; jj < 5; ++jj) a3[i][jj] = (f32x4){0, 0, 0, 0};
    checkG(0);
    STAMP(3, 0);
    RAW_BAR();
    const unsigned wlb = sb + kg * 5120 + lane * 16;
    auto wreg = [&](int s) -> unsigned { const int t = s < 7 ? s : s - 7; return t < 4 ? L_WA + t * 20480 : (t - 4) * 20480; };
    u32x4 wc[5];
#pragma unroll
    for (int jj = 0; jj < 5; ++jj) wc[jj] = lds_r128(wlb + wreg(0) + jj * 1024);
#pragma unroll
    for (int s = 0; s < 12; ++s) {
      if (s == 3) { checkG(1); RAW_BAR(); }
      if (s == 4) { loadG(2); SCHED(); }
      if (s == 8) checkG(2);
      if (s == 5) {
        RAW_BAR();
        if (!isC) {
#pragma unroll
          for (int t = 0; t < 5; ++t)
#pragma unroll
            for (int jj = 0; jj < 5; ++jj) lds_w128(wlb + wreg(7 + t) + jj * 1024, wh[t][jj]);
          LGKM0_BAR();
        }
      }
      SCHED();
#pragma unroll
      for (int jj = 0; jj < 5; ++jj) {
#pragma unroll
        for (int i = 0; i < 2; ++i) a3[i][jj] = mfma16(as_bf(wc[jj]), as_bf(hf[(s >> 2) & 1][s & 3][i]), a3[i][jj]);
        SCHED();
        if (s + 1 < 12 && s != 6) { wc[jj] = lds_r128(wlb + wreg(s + 1) + jj * 1024); SCHED(); }
      }
      if (s == 6) {
        if (isC) RAW_BAR();
#pragma unroll
        for (int jj = 0; jj < 5; ++jj) wc[jj] = lds_r128(wlb + wreg(7) + jj * 1024);
      }
    }
    STAMP(3, 1);
    LGKM0_BAR();
#pragma unroll
    for (int i = 0; i < 2; ++i)
#pragma unroll
      for (int jj = 0; jj < 5; ++jj) lds_w128f(sb + L_WA + (kg * 20 + (th * 2 + i) * 5 + jj) * 1024 + lane * 16, a3[i][jj]);
    LGKM0_BAR();
    STAMP(3, 2);
#pragma unroll
    for (int r = 0; r < 3; ++r) {
      const int tl = w + 8 * r;
      if (tl < 20) {
        const int tt = tl / 5, dt = tl - 5 * tt;
        const unsigned a = sb + L_WA + tl * 1024 + lane * 16;
        const f32x4 o = (lds_r128f(a) + lds_r128f(a + 20480)) + (lds_r128f(a + 40960) + lds_r128f(a + 61440));
        const int row = 16 * dt + 4 * g + q;
        const s16x4 hr = tr_read(sb + L_RES + row * 128 + ((tt ^ ((row >> 1) & 3)) * 32) + p * 8);
        const u32x2 hu = __builtin_bit_cast(u32x2, hr);
        const unsigned o01 = pk_bf16(o[0], o[1]), o23 = pk_bf16(o[2], o[3]);
        u32x2 ov = {pk_bf16(bf_lo(hu[0]) + bf_lo(o01), bf_hi(hu[0]) + bf_hi(o01)),
                    pk_bf16(bf_lo(hu[1]) + bf_lo(o23), bf_hi(hu[1]) + bf_hi(o23))};
        *(u32x2*)(out + (size_t)(128 * tb + 64 * half + 16 * tt + t16) * D + 80 * db + 16 * dt + 4 * g) = ov;
      }
    }
    STAMP(3, 3);
  }
}

extern "C" void kernel_launch(void* const* d_in, const int* in_sizes, int n_in, void* d_out, int out_size,
                              void* d_ws, size_t ws_size, hipStream_t stream) {
  const u16* q = (const u16*)d_in[0];
  const u16* k = (const u16*)d_in[1];
  const u16* v = (const u16*)d_in[2];
  const u16* shk = (const u16*)d_in[3];
  const u16* ssmk = (const u16*)d_in[4];
  const u16* ssmD = (const u16*)d_in[5];
  const u16* Wg = (const u16*)d_in[7];
  const u16* Wu = (const u16*)d_in[8];
  const u16* Wd = (const u16*)d_in[9];
  char* ws = (char*)d_ws;
  k0_prep<<<K0GRID, 256, 0, stream>>>(q, k, v, shk, ws);
  kf_main<3><<<256, 512, 0, stream>>>(ssmk, ssmD, Wg, Wu, Wd, ws, (u16*)d_out);
}
```

```cpp
#include <hip/hip_runtime.h>
#include <stdint.h>

typedef unsigned short u16;
typedef short s16x4 __attribute__((ext_vector_type(4)));
typedef short s16x8 __attribute__((ext_vector_type(8)));
typedef __bf16 bf16x8 __attribute__((ext_vector_type(8)));
typedef __bf16 bf16x2 __attribute__((ext_vector_type(2)));
typedef float f32x2 __attribute__((ext_vector_type(2)));
typedef float f32x4 __attribute__((ext_vector_type(4)));
typedef unsigned u32x2 __attribute__((ext_vector_type(2)));
typedef unsigned u32x4 __attribute__((ext_vector_type(4)));
#define LDS_AS __attribute__((address_space(3)))
#define DEVI __device__ __forceinline__

constexpr int S = 2048, D = 640, F = 1536;
constexpr float EPS = 1e-5f;
constexpr unsigned MARK = 0xffffffffu;
constexpr int SPIN_CAP = 1 << 14;

constexpr unsigned WS_UT = 0;
constexpr unsigned WS_XT = WS_UT + D * S * 2;
constexpr unsigned WS_HT = WS_XT + D * S * 2;
constexpr unsigned WS_ST = WS_HT + D * S * 2;
constexpr unsigned WS_CNT = WS_ST + S * 8;
constexpr unsigned WS_WD = WS_CNT + 8192;
constexpr unsigned WS_SQ = WS_WD + D * F * 2;
constexpr unsigned WS_HH = WS_SQ + S * 4;
constexpr unsigned WS_END = WS_HH + S * F * 2;

DEVI float bf_lo(unsigned u) { return __builtin_bit_cast(float, u << 16); }
DEVI float bf_hi(unsigned u) { return __builtin_bit_cast(float, u & 0xffff0000u); }
DEVI unsigned pk_bf16(float a, float b) {
  f32x2 v = {a, b};
  bf16x2 r = __builtin_convertvector(v, bf16x2);
  return __builtin_bit_cast(unsigned, r);
}
DEVI float rnd_bf16(float a) { return bf_lo(pk_bf16(a, a)); }
DEVI unsigned unmark(unsigned u) { return u < 0xfffffffeu ? u : 0xfffffffeu; }
DEVI s16x4 tr_read(unsigned lds_byte) {
  return __builtin_amdgcn_ds_read_tr16_b64_v4i16((LDS_AS s16x4*)(uintptr_t)lds_byte);
}
DEVI s16x4 lds_r64(unsigned a) { return *(volatile LDS_AS const s16x4*)(uintptr_t)a; }
DEVI u32x4 lds_r128(unsigned a) { return *(LDS_AS const u32x4*)(uintptr_t)a; }
DEVI void lds_w128(unsigned a, u32x4 v) { *(LDS_AS u32x4*)(uintptr_t)a = v; }
DEVI f32x4 lds_r128f(unsigned a) { return *(LDS_AS const f32x4*)(uintptr_t)a; }
DEVI void lds_w128f(unsigned a, f32x4 v) { *(LDS_AS f32x4*)(uintptr_t)a = v; }
DEVI bf16x8 cat8(s16x4 a, s16x4 b) {
  s16x8 ab = __builtin_shufflevector(a, b, 0, 1, 2, 3, 4, 5, 6, 7);
  return __builtin_bit_cast(bf16x8, ab);
}
DEVI bf16x8 as_bf(u32x4 v) { return __builtin_bit_cast(bf16x8, v); }
DEVI f32x4 mfma16(bf16x8 a, bf16x8 b, f32x4 c) { return __builtin_amdgcn_mfma_f32_16x16x32_bf16(a, b, c, 0, 0, 0); }
DEVI unsigned lds_addr(const void* p) { return (unsigned)(uintptr_t)(LDS_AS const char*)p; }
DEVI void glds16_asm(const void* gsrc, unsigned lds_dst) {
  unsigned keep;
  asm volatile("s_mov_b32 %0, m0\n\ts_mov_b32 m0, %2\n\ts_nop 0\n\tglobal_load_lds_dwordx4 %1, off sc1\n\ts_mov_b32 m0, %0"
               : "=&s"(keep) : "v"(gsrc), "s"(lds_dst) : "memory");
}
DEVI void glds16_s(const void* sbase, unsigned voff, unsigned lds_dst) {
  unsigned keep;
  asm volatile("s_mov_b32 %0, m0\n\ts_mov_b32 m0, %3\n\ts_nop 0\n\tglobal_load_lds_dwordx4 %1, %2\n\ts_mov_b32 m0, %0"
               : "=&s"(keep) : "v"(voff), "s"(sbase), "s"(lds_dst) : "memory");
}
DEVI void glds16_s3(const void* sbase, unsigned v0, unsigned v1, unsigned v2, unsigned lds_dst) {
  unsigned keep;
  asm volatile("s_mov_b32 %0, m0\n\ts_mov_b32 m0, %5\n\ts_nop 0\n\tglobal_load_lds_dwordx4 %1, %4\n\t"
               "global_load_lds_dwordx4 %2, %4 offset:1024\n\tglobal_load_lds_dwordx4 %3, %4 offset:2048\n\ts_mov_b32 m0, %0"
               : "=&s"(keep) : "v"(v0), "v"(v1), "v"(v2), "s"(sbase), "s"(lds_dst) : "memory");
}
typedef __amdgpu_buffer_rsrc_t rsrc_t;
DEVI u32x4 ld_dev(rsrc_t rs, unsigned voff, unsigned soff) { return __builtin_amdgcn_raw_buffer_load_b128(rs, voff, soff, 16); }
DEVI u32x4 ld_pl(rsrc_t rs, unsigned voff, unsigned soff) { return __builtin_amdgcn_raw_buffer_load_b128(rs, voff, soff, 0); }
DEVI void st_dev(rsrc_t rs, unsigned voff, unsigned soff, u32x4 v) { __builtin_amdgcn_raw_buffer_store_b128(v, rs, voff, soff, 16); }
DEVI unsigned max4(u32x4 v) { return max(max(v[0], v[1]), max(v[2], v[3])); }
DEVI unsigned max16(const u32x4 (&v)[4]) { return max(max(max4(v[0]), max4(v[1])), max(max4(v[2]), max4(v[3]))); }
template <int CTRL> DEVI float dpp_f(float x) {
  return __builtin_bit_cast(float, __builtin_amdgcn_mov_dpp(__builtin_bit_cast(int, x), CTRL, 0xf, 0xf, true));
}
template <int I> struct IC { static constexpr int v = I; };
template <int I, int N, class Fn> DEVI void static_for(Fn&& f) {
  if constexpr (I < N) { f(IC<I>{}); static_for<I + 1, N>(f); }
}
DEVI float f_of(unsigned u) { return __builtin_bit_cast(float, u); }
DEVI unsigned u_of(float f) { return __builtin_bit_cast(unsigned, f); }

#define STAMP(k, i) do {} while (0)
#define TR(i) do {} while (0)
#define CBAR() asm volatile("" ::: "memory")
#define SCHED() __builtin_amdgcn_sched_barrier(0)
#define RAW_BAR() do { CBAR(); __builtin_amdgcn_s_barrier(); CBAR(); SCHED(); } while (0)
#define LGKM0_BAR() do { asm volatile("s_waitcnt lgkmcnt(0)" ::: "memory"); __builtin_amdgcn_s_barrier(); CBAR(); SCHED(); } while (0)
#define SETTLE(BAD, RELOAD) do { if (__builtin_expect(__any(BAD), 0)) { int sp_ = 0; \
    _Pragma("clang loop unroll(disable)") do { __builtin_amdgcn_s_sleep(2); RELOAD; } while (__any(BAD) && ++sp_ < SPIN_CAP); } } while (0)

#define K0AUX 0
#define NOPOISON 0
#define K0GRID (K0_TR + K0_ST)
__global__ void k_tail(char* ws) { if (blockIdx.x == 9999) ws[0] = 1; }
constexpr int K0_TR = 320, K0_ST = 128;
__global__ __launch_bounds__(256) void k0_prep(const u16* __restrict__ x, const u16* __restrict__ kin,
                                               const u16* __restrict__ vin, const u16* __restrict__ shk,
                                               char* __restrict__ ws) {
  __shared__ __attribute__((aligned(16))) char smem[2 * 64 * 144];
  const int tid = threadIdx.x, lane = tid & 63;
  const int w = __builtin_amdgcn_readfirstlane(tid >> 6);
  const int b = blockIdx.x;
  const int g = lane >> 4, q = (lane >> 2) & 3, p = lane & 3;
  const rsrc_t rs0 = __builtin_amdgcn_make_buffer_rsrc(ws, 0, WS_END, 0x00020000);
#define K0ST(off, v) __builtin_amdgcn_raw_buffer_store_b128(v, rs0, off, 0, K0AUX)
  STAMP(0, 0);
  if (b < K0_TR) {
    const int tt = b & 31, cg = b >> 5;
    const int r2 = tid >> 3, cc = tid & 7;
    const int t0 = 64 * tt + 2 * r2, c0 = 64 * cg + 8 * cc;
    u32x4 kr[5], vr[2], xr[2], sw[4];
#pragma unroll
    for (int i = 0; i < 5; ++i) {
      const int t = t0 - 3 + i;
      const int tc = t < 0 ? 0 : t;
      kr[i] = *(const u32x4*)(kin + (size_t)tc * D + c0);
      if (t < 0) kr[i] = (u32x4){0, 0, 0, 0};
    }
#pragma unroll
    for (int i = 0; i < 2; ++i) {
      vr[i] = *(const u32x4*)(vin + (size_t)(t0 + i) * D + c0);
      xr[i] = *(const u32x4*)(x + (size_t)(t0 + i) * D + c0);
    }
#pragma unroll
    for (int i = 0; i < 4; ++i) sw[i] = *(const u32x4*)(shk + (size_t)c0 * 4 + 8 * i);
#pragma unroll
    for (int i = 0; i < 2; ++i) if (!NOPOISON) K0ST(WS_HT + b * 8192 + i * 4096 + tid * 16, ((u32x4){MARK, MARK, MARK, MARK}));
    char* su = smem;
    char* sx = smem + 64 * 144;
#pragma unroll
    for (int i = 0; i < 2; ++i) {
      u32x4 o;
#pragma unroll
      for (int cp = 0; cp < 4; ++cp) {
        float r[2];
#pragma unroll
        for (int hh = 0; hh < 2; ++hh) {
          const unsigned w01 = sw[cp][2 * hh], w23 = sw[cp][2 * hh + 1];
          float kk[4];
#pragma unroll
          for (int j = 0; j < 4; ++j) kk[j] = hh ? bf_hi(kr[i + 3 - j][cp]) : bf_lo(kr[i + 3 - j][cp]);
          float a = kk[3] * bf_hi(w23);
          a = fmaf(kk[2], bf_lo(w23), a);
          a = fmaf(kk[1], bf_hi(w01), a);
          a = fmaf(kk[0], bf_lo(w01), a);
          r[hh] = a;
        }
        const unsigned ks = pk_bf16(r[0], r[1]);
        o[cp] = pk_bf16(bf_lo(ks) * bf_lo(vr[i][cp]), bf_hi(ks) * bf_hi(vr[i][cp]));
      }
      *(u32x4*)(su + (2 * r2 + i) * 144 + cc * 16) = o;
      *(u32x4*)(sx + (2 * r2 + i) * 144 + cc * 16) = xr[i];
    }
    STAMP(0, 1);
    __syncthreads();
    STAMP(0, 2);
    const int c = 64 * cg + 16 * w + (lane & 15);
    const unsigned colb = (16 * w + 4 * p) * 2;
#pragma unroll
    for (int si = 0; si < 2; ++si) {
      const int ci = 4 * si + g;
      {
        const int rho = ci >> 1, half = ci & 1;
        const int row = 4 * (8 * half + q) + rho;
        const unsigned a = lds_addr(su) + row * 144 + colb;
        const bf16x8 v = cat8(tr_read(a), tr_read(a + 16 * 144));
        K0ST(WS_UT + ((c * 32 + tt) * 64 + ci * 8) * 2, __builtin_bit_cast(u32x4, v));
      }
      {
        const int row = 8 * ci + q;
        const unsigned a = lds_addr(sx) + row * 144 + colb;
        const bf16x8 v = cat8(tr_read(a), tr_read(a + 4 * 144));
        K0ST(WS_XT + (c * S + 64 * tt + ci * 8) * 2, __builtin_bit_cast(u32x4, v));
      }
    }
  } else {
    const int row = (b - K0_TR) * 16 + 4 * w + (lane >> 4), sub = lane & 15;
    if (b == K0_TR && tid < 24) *(unsigned*)(ws + WS_CNT + tid * 256) = 0u;
    u32x4 xr[5];
#pragma unroll
    for (int i = 0; i < 5; ++i) xr[i] = *(const u32x4*)(x + (size_t)row * D + (sub + 16 * i) * 8);
    float s = 0.f;
#pragma unroll
    for (int i = 0; i < 5; ++i)
#pragma unroll
      for (int j = 0; j < 4; ++j) s += bf_lo(xr[i][j]) + bf_hi(xr[i][j]);
    s += dpp_f<0xB1>(s); s += dpp_f<0x4E>(s); s += dpp_f<0x124>(s); s += dpp_f<0x128>(s);
    const float mean = s * (1.0f / D);
    float qq = 0.f;
#pragma unroll
    for (int i = 0; i < 5; ++i)
#pragma unroll
      for (int j = 0; j < 4; ++j) {
        const float a = bf_lo(xr[i][j]) - mean, c = bf_hi(xr[i][j]) - mean;
        qq = fmaf(a, a, qq);
        qq = fmaf(c, c, qq);
      }
    qq += dpp_f<0xB1>(qq); qq += dpp_f<0x4E>(qq); qq += dpp_f<0x124>(qq); qq += dpp_f<0x128>(qq);
    const float var = rnd_bf16(qq * (1.0f / D));
    const float sd = rnd_bf16(sqrtf(rnd_bf16(var + EPS)));
    if (sub == 0) {
      f32x2 st = {rnd_bf16(mean), 1.0f / sd};
      *(f32x2*)(ws + WS_ST + (size_t)row * 8) = st;
      *(unsigned*)(ws + WS_SQ + (size_t)row * 4) = MARK;
    }
  }
  STAMP(0, 3);
}

#define STATS_MODE 1
constexpr int NCONV = 214;
constexpr int CH_U = 0, CH_K = 4608, CH_Y = CH_K + 4128, CH_C = CH_Y + 8192, CH_BYTES = CH_C + 8192;
constexpr int WSLOT = 12320, WUP = 6176;
constexpr int L_WA7 = 49152, L_RES = 61504, L_ST = L_RES + 10240, L_CS = L_ST + 1024, L_SQ = L_CS + 768, L_CNT = 76000, L_RDY = L_CNT + 16, L_DONE = L_RDY + 48, L_WA = 76800, L_P3 = L_WA;
#define DIAG 0
#define WDSCHED {0, 1, 3, 5, 7, 7, 7, 8, 9, 10, 10}
#define P2PRIO 1
#define NH 5
#define PRE 4
#define NWH 10
constexpr int WREG = 4;

DEVI float addf(float a, float b) { float r = a + b; asm("" : "+v"(r)); return r; }
DEVI void conv_store(unsigned base, int ct, int a16, int g, const f32x4 (&z)[4]) {
  f32x4 y = z[0], c = z[3];
  y[1] = addf(y[1], z[1][0]);
  y[2] = addf(addf(y[2], z[1][1]), z[2][0]);
  y[3] = addf(addf(y[3], z[1][2]), addf(z[2][1], z[3][0]));
  c[1] = addf(addf(c[1], z[1][3]), z[2][2]);
  c[2] = addf(c[2], z[2][3]);
  const unsigned o = base + ct * 1024 + g * 256 + ((a16 ^ ((g >> 1) << 3)) * 16);
  lds_w128f(o + CH_Y, y);
  lds_w128f(o + CH_C, c);
}
template <int TOP, int NT, int EX, int NCH>
DEVI void conv_run(unsigned base0, int lane) {
  const int a16 = lane & 15, g = lane >> 4, q = (lane >> 2) & 3, pp = lane & 3;
  const unsigned ab = base0 + CH_U + 128 + 2 * (4 * a16 - 4 - 8 * g) - 64;
  const unsigned aa = base0 + CH_K + 32 + 2 * (32 * g - 4 * q + 4 * pp);
  constexpr int LOW = TOP - NT + 1, NS = 2 * TOP + 2, NA = 2 * TOP + 2;
  f32x4 acc[NT][4], acc0[4];
  bf16x8 A[2][NA], B[3][4];
  auto loadA = [&](int ch, int n) { A[ch & 1][n] = cat8(tr_read(aa + ch * CH_BYTES + 256 * n + 32), tr_read(aa + ch * CH_BYTES + 256 * n)); };
  auto loadStep = [&](int gs) {
    const int ch = gs / NS, st = gs % NS;
    if (st < 2) {
#pragma unroll
      for (int t = NT - 1; t >= 0; --t) loadA(ch, 2 * (LOW + t) + 1 - st);
    }
#pragma unroll
    for (int r = 0; r < 4; ++r) B[gs % 3][r] = cat8(lds_r64(ab + ch * CH_BYTES + 64 * st + r * 1152), lds_r64(ab + ch * CH_BYTES + 64 * st + r * 1152 + 8));
    if (st >= 2 && 2 * LOW + 1 - st > 2 * EX + 1) loadA(ch, 2 * LOW + 1 - st);
    if (2 * EX + 1 - st >= 0) loadA(ch, 2 * EX + 1 - st);
  };
  loadStep(0);
  loadStep(1);
  SCHED();
#pragma unroll
  for (int gs = 0; gs < NCH * NS; ++gs) {
    const int ch = gs / NS, st = gs % NS;
    if (gs + 2 < NCH * NS) loadStep(gs + 2);
#pragma unroll
    for (int t = NT - 1; t >= 0; --t) {
      const int n = 2 * (LOW + t) + 1 - st;
      if (n >= 0) {
#pragma unroll
        for (int r = 0; r < 4; ++r) acc[t][r] = mfma16(A[ch & 1][n], B[gs % 3][r], st == 0 ? (f32x4){0, 0, 0, 0} : acc[t][r]);
      }
    }
    if (2 * EX + 1 - st >= 0) {
#pragma unroll
      for (int r = 0; r < 4; ++r) acc0[r] = mfma16(A[ch & 1][2 * EX + 1 - st], B[gs % 3][r], st == 0 ? (f32x4){0, 0, 0, 0} : acc0[r]);
    }
    if (st == 2 * EX + 2) conv_store(base0 + ch * CH_BYTES, EX, a16, g, acc0);
    if (st >= 2 * LOW + 2 && !(st & 1)) conv_store(base0 + ch * CH_BYTES, (st - 2) / 2, a16, g, acc[(st - 2) / 2 - LOW]);
#pragma unroll
    for (int i = 0; i < 20; ++i) {
      __builtin_amdgcn_sched_group_barrier(0x8, 1, 0);
      __builtin_amdgcn_sched_group_barrier(0x100, 1, 0);
      __builtin_amdgcn_sched_group_barrier(0x2, 1, 0);
    }
    SCHED();
    if (st == NS - 1) { conv_store(base0 + ch * CH_BYTES, TOP, a16, g, acc[NT - 1]); SCHED(); }
  }
}

template <int PH>
__global__ __launch_bounds__(512, 2) void kf_main(const u16* __restrict__ ssmk, const u16* __restrict__ ssmD,
                                                  const u16* __restrict__ Wg, const u16* __restrict__ Wu,
                                                  const u16* __restrict__ Wd, char* __restrict__ ws,
                                                  u16* __restrict__ out) {
  __shared__ __attribute__((aligned(16))) char smem[163840];
  const int tid = threadIdx.x, lane = tid & 63;
  const int w = __builtin_amdgcn_readfirstlane(tid >> 6);
  const int b = blockIdx.x, xcd = b & 7, rr = b >> 3;
  const unsigned sb = lds_addr(smem);
  const rsrc_t rs = __builtin_amdgcn_make_buffer_rsrc(ws, 0, WS_END, 0x00020000);
  const int g = lane >> 4, q = (lane >> 2) & 3, p = lane & 3, t16 = lane & 15;
  const int tb = 4 * (xcd & 3) + (rr & 3), nb = 8 * (xcd >> 2) + (rr >> 2);
  const int half = nb & 1, db = nb >> 1;
  const bool isC = w < 4;
  const int hw = w & 3;
  const bool conv = b < NCONV;
  const int nch = conv ? min(3, D - 3 * b) : 0;
  LDS_AS unsigned* cnt = (LDS_AS unsigned*)(uintptr_t)(sb + L_CNT);
  STAMP(1, 0);
  unsigned pfacc = 0, pfacc2 = 0;
  if (w == 3) {
    unsigned len;
    asm volatile("kf_code_begin:\n\ts_mov_b32 %0, kf_code_end - kf_code_begin" : "=s"(len));
    const char* pc = (const char*)__builtin_amdgcn_s_getpc();
    const unsigned o = (rr * 2) * 1024 + lane * 16;
    const unsigned v0 = *(const __attribute__((address_space(1))) unsigned*)(pc + (o + 64 < len ? o : 0));
    const unsigned v1 = *(const __attribute__((address_space(1))) unsigned*)(pc + (o + 1088 < len ? o + 1024 : 0));
    pfacc = v0; pfacc2 = v1;
  }
  if (tid < 32) cnt[tid] = 0;

  if (!conv) {
    const int u = (b - NCONV) * 8 + w;
    if (u < 240) {
      const int ks2 = u / 5, dq = u % 5;
      constexpr int RS = 288;
      const unsigned sm = sb + w * 32 * RS;
      u32x4 v8[8];
#pragma unroll
      for (int i = 0; i < 8; ++i) v8[i] = *(const u32x4*)(Wd + (size_t)(32 * ks2 + 4 * i + g) * D + 128 * dq + 8 * t16);
#pragma unroll
      for (int i = 0; i < 8; ++i) lds_w128(sm + (4 * i + g) * RS + t16 * 16, v8[i]);
      CBAR();
#pragma unroll
      for (int t = 0; t < 8; ++t) {
        const int dt = 8 * dq + t;
        const unsigned a = sm + (8 * g + q) * RS + t * 32 + p * 8;
        const bf16x8 v = cat8(tr_read(a), tr_read(a + 4 * RS));
        st_dev(rs, lane * 16, WS_WD + (((dt / 5) * 48 + ks2) * 5 + (dt % 5)) * 1024, __builtin_bit_cast(u32x4, v));
      }
    }
    asm volatile("s_waitcnt vmcnt(0)" ::: "memory");
    __syncthreads();
    if (tid < 8) __hip_atomic_fetch_add((__attribute__((address_space(1))) unsigned*)(ws + WS_CNT + tid * 256), 1u, __ATOMIC_RELAXED, __HIP_MEMORY_SCOPE_AGENT);
  }

  f32x4 acc[3][8];
  const int kg = w & 3;
  const unsigned wdoff = WS_WD + (db * 48 + 12 * kg) * 5 * 1024;
  u32x4 wh[NH][5];
  u32x4 wx[5][5];

  if (isC) {
    u32x4 xv[3], st4[4];
    float dval[3];
    if (conv) {
#pragma unroll
      for (int i = 0; i < 3; ++i) {
        const int ch = min(3 * b + i, D - 1);
        xv[i] = ld_pl(rs, tid * 16, WS_XT + ch * 4096);
        dval[i] = bf_lo(ssmD[ch]);
      }
#pragma unroll
      for (int i = 0; i < 4; ++i) st4[i] = ld_pl(rs, tid * 64 + i * 16, WS_ST);
      if (w < 3) {
        const unsigned cb = sb + w * CH_BYTES;
        if (lane < 32) lds_w128(cb + CH_U + (lane >> 3) * 1152 + (lane & 7) * 16, (u32x4){0, 0, 0, 0});
        else if (lane < 34) lds_w128(cb + CH_K + (lane - 32) * 16, (u32x4){0, 0, 0, 0});
      }
    }
    LGKM0_BAR();
    asm volatile("" :: "v"(pfacc), "v"(pfacc2));
    STAMP(1, 1);
    if (conv) {
      if (w < 3) {
        conv_run<7, 4, 1, 1>(sb + w * CH_BYTES, lane);
      } else {
        conv_run<3, 2, 0, 3>(sb, lane);
      }
      STAMP(1, 2);
      asm volatile("s_waitcnt lgkmcnt(0)" ::: "memory");
      if (lane == 0) __hip_atomic_fetch_add(cnt, 1u, __ATOMIC_RELAXED, __HIP_MEMORY_SCOPE_WORKGROUP);
#pragma clang loop unroll(disable)
      for (int sp = 0; sp < (1 << 20); ++sp) {
        if (__builtin_amdgcn_readfirstlane(*(volatile LDS_AS unsigned*)cnt) >= 4u) break;
        __builtin_amdgcn_s_sleep(1);
      }
      CBAR();
      STAMP(1, 3);
      {
        auto pos = [&](int blk) -> unsigned { const int gg = blk & 3; return (blk >> 6) * 1024 + gg * 256 + ((((blk >> 2) & 15) ^ ((gg >> 1) << 3)) * 16); };
        const unsigned p0 = sb + pos(2 * tid), p1 = sb + pos(2 * tid + 1), pm = sb + pos(tid ? 2 * tid - 1 : 0);
        f32x4 y0[3], y1[3], c0[3], c1[3];
        unsigned uu[3][4];
#pragma unroll
        for (int lc = 0; lc < 3; ++lc) {
          y0[lc] = lds_r128f(p0 + lc * CH_BYTES + CH_Y);
          c0[lc] = lds_r128f(pm + lc * CH_BYTES + CH_C);
          y1[lc] = lds_r128f(p1 + lc * CH_BYTES + CH_Y);
          c1[lc] = lds_r128f(p0 + lc * CH_BYTES + CH_C);
#pragma unroll
          for (int r = 0; r < 4; ++r) uu[lc][r] = *(LDS_AS const unsigned*)(uintptr_t)(sb + lc * CH_BYTES + CH_U + r * 1152 + 128 + tid * 4);
        }
        SCHED();
#pragma unroll
        for (int lc = 0; lc < 3; ++lc) {
          float h[8];
#pragma unroll
          for (int e = 0; e < 8; ++e) {
            float y = e < 4 ? y0[lc][e & 3] : y1[lc][e & 3];
            if ((e & 3) < 3) y += e < 4 ? (tid ? c0[lc][(e & 3) + 1] : 0.f) : c1[lc][(e & 3) + 1];
            const float u = e < 4 ? bf_lo(uu[lc][e & 3]) : bf_hi(uu[lc][e & 3]);
            const float xx = (e & 1) ? bf_hi(xv[lc][e >> 1]) : bf_lo(xv[lc][e >> 1]);
            const float m = f_of(st4[e >> 1][2 * (e & 1)]), rsd = f_of(st4[e >> 1][2 * (e & 1) + 1]);
            h[e] = fmaf((xx - m) * rsd, fmaf(u, dval[lc], y), xx);
          }
          u32x4 o = {unmark(pk_bf16(h[0], h[1])), unmark(pk_bf16(h[2], h[3])), unmark(pk_bf16(h[4], h[5])), unmark(pk_bf16(h[6], h[7]))};
          st_dev(rs, lc < nch ? tid * 16 : 0x80000000u, WS_HT + min(3 * b + lc, D - 1) * 4096, o);
          SCHED();
        }
      }
    }
    STAMP(1, 4);
    LGKM0_BAR();
    unsigned wa[2][3];
    {
      const int r = 4 * g + q;
#pragma unroll
      for (int jj = 0; jj < 3; ++jj) {
        wa[0][jj] = sb + L_WA7 + (p & 1) * WUP + (r >> 2) * 768 + ((12 * (r & 3) + 3 * w + jj + ((r >> 2) & 1)) % 48) * 16 + (p >> 1) * 8;
        wa[1][jj] = wa[0][jj] + (L_WA + 3 * WSLOT - L_WA7);
        asm volatile("" : "+v"(wa[0][jj]), "+v"(wa[1][jj]));
      }
    }
    auto wadr = [&](int slot, int jj) -> unsigned {
      return slot == 7 ? wa[0][jj] : slot < 3 ? wa[0][jj] + (L_WA - L_WA7 + slot * WSLOT) : wa[1][jj] + (slot - 3) * WSLOT;
    };
    auto loadW = [&](int slot, bf16x8 (&dst)[3]) {
#pragma unroll
      for (int jj = 0; jj < 3; ++jj) dst[jj] = cat8(tr_read(wadr(slot, jj)), tr_read(wadr(slot, jj) + 3072));
    };
    bf16x8 wr[WREG][3], wl[3];
#pragma unroll
    for (int s = 0; s < WREG; ++s) loadW(s, wr[s]);
    LGKM0_BAR();
    u32x4 ones_u = {0x3f803f80u, 0x3f803f80u, 0x3f803f80u, 0x3f803f80u};
    asm volatile("" : "+v"(ones_u));
    const bf16x8 ones = as_bf(ones_u);
    f32x4 cs[3] = {{0, 0, 0, 0}, {0, 0, 0, 0}, {0, 0, 0, 0}};
#pragma unroll
    for (int s = 0; s < WREG; ++s)
#pragma unroll
      for (int jj = 0; jj < 3; ++jj) cs[jj] = mfma16(wr[s][jj], ones, cs[jj]);
    RAW_BAR();
#pragma unroll
    for (int s = WREG; s < 8 + WREG; ++s) {
      loadW(s & 7, wl);
#pragma unroll
      for (int jj = 0; jj < 3; ++jj) cs[jj] = mfma16(wl[jj], ones, cs[jj]);
    }
#pragma unroll
    for (int jj = 0; jj < 3; ++jj) {
      lds_w128f(sb + L_CS + w * 192 + jj * 64 + g * 16, cs[jj]);
      cs[jj] = (f32x4){0, 0, 0, 0};
    }
    SCHED();
    unsigned ta[8];
    {
      const unsigned lb = sb + (4 * g + q) * 256 + 32 * (4 * (g & 1) + q) + 16 * (p >> 1) + 8 * (p & 1);
#pragma unroll
      for (int i = 0; i < 8; ++i) ta[i] = lb ^ (32 * ((i + 2 * w) & 7));
    }
#pragma unroll
    for (int jj = 0; jj < 3; ++jj)
#pragma unroll
      for (int i = 0; i < 8; ++i) acc[jj][i] = (f32x4){0, 0, 0, 0};
    s16x4 af[3][4][2], wq[2][3][2];
    auto hoff = [&](int hs) -> unsigned { const int ks = hs >> 1; return ((ks >> 1) % 3) * 16384 + (ks & 1) * 8192; };
    f32x4 sacc[2] = {{0, 0, 0, 0}, {0, 0, 0, 0}};
    auto waitReady = [&](unsigned f, int c) {
      if (__builtin_expect(__builtin_amdgcn_readfirstlane(f) < 4u, 0)) {
        int sp = 0;
        TR(2 * c);
#pragma clang loop unroll(disable)
        do {
          __builtin_amdgcn_s_sleep(1);
          f = *(volatile LDS_AS const unsigned*)(uintptr_t)(sb + L_RDY + 4 * c);
        } while (__builtin_amdgcn_readfirstlane(f) < 4u && ++sp < (1 << 22));
        TR(2 * c + 1);
      }
      CBAR();
      SCHED();
    };
    unsigned rdy = 0;
    waitReady(rdy, 0);
    STAMP(2, 1);
#pragma unroll
    for (int i = 0; i < 8; ++i) { af[i >> 2][i & 3][0] = tr_read(ta[i]); af[i >> 2][i & 3][1] = tr_read(ta[i] + 4096); }
    SCHED();
    static_for<0, 40>([&](auto HS) {
      constexpr int hs = HS.v, ks = hs >> 1, cur = hs & 1, cb = hs % 3, nx = (hs + 2) % 3;
      if (hs + 2 < 40 && ((hs + 2) & 3) == 0) waitReady(rdy, (hs + 2) / 4);
      if ((hs & 3) == 1 && hs + 3 < 40) { rdy = *(volatile LDS_AS const unsigned*)(uintptr_t)(sb + L_RDY + 4 * ((hs + 3) / 4)); SCHED(); }
      if ((hs & 3) == 0 && hs >= 4 && hs <= 28) {
        *(volatile LDS_AS unsigned*)(uintptr_t)(sb + L_DONE + w * 4) = hs / 4;
        SCHED();
      }
      constexpr bool refill = !cur && ks + 1 >= WREG && ks + 1 < 20;
      constexpr int wo = (ks + 1) & 7;
      int gap = 0;
      auto filler = [&]() {
        const int n = gap++;
        if (n < 8) {
          if (hs + 2 < 40) af[nx][n >> 1][n & 1] = tr_read(ta[4 * cur + (n >> 1)] + hoff(hs + 2) + (n & 1) * 4096);
        } else if (refill && n < 14) {
          wq[(ks + 1) & 1][(n - 8) >> 1][n & 1] = tr_read(wadr(wo, (n - 8) >> 1) + (n & 1) * 3072);
        }
        SCHED();
      };
#pragma unroll
      for (int jj = 0; jj < 3; ++jj) {
        const bf16x8 wv = ks < WREG ? wr[ks < WREG ? ks : 0][jj] : cat8(wq[ks & 1][jj][0], wq[ks & 1][jj][1]);
#pragma unroll
        for (int i = 0; i < 4; ++i) {
          acc[jj][4 * cur + i] = mfma16(wv, cat8(af[cb][i][0], af[cb][i][1]), acc[jj][4 * cur + i]);
          SCHED();
          filler();
        }
        if (ks >= 8 + WREG && cur) { cs[jj] = mfma16(wv, ones, cs[jj]); SCHED(); }
        if (!cur && jj < 2) { sacc[jj] = mfma16(ones, cat8(af[cb][jj][0], af[cb][jj][1]), sacc[jj]); SCHED(); filler(); }
      }
    });
#pragma unroll
    for (int i = 0; i < 2; ++i) *(LDS_AS float*)(uintptr_t)(sb + L_ST + ((2 * w + i) * 16 + t16) * 4) = sacc[i][0] * (1.0f / D);
    STAMP(2, 2);
    LGKM0_BAR();
    STAMP(2, 4);
    int lz = threadIdx.x;
    asm volatile("" : "+v"(lz));
    const int lane = lz & 63, g = lane >> 4, t16 = lane & 15;
#pragma unroll
    for (int jj = 0; jj < 3; ++jj) cs[jj] += lds_r128f(sb + L_CS + w * 192 + jj * 64 + g * 16);
    float st[8];
#pragma unroll
    for (int i = 0; i < 8; ++i) st[i] = rnd_bf16(*(LDS_AS const float*)(uintptr_t)(sb + L_ST + (((i + 2 * w) & 7) * 16 + t16) * 4));
    SCHED();
#pragma unroll
    for (int hf = 0; hf < 2; ++hf) {
      const unsigned vo = (tb * 8 + ((4 * hf + 2 * (g >> 1) + (g & 1) + 2 * w) & 7)) * 49152 + t16 * 16;
#pragma unroll
      for (int jj = 0; jj < 3; ++jj) {
        unsigned X[2][2];
#pragma unroll
        for (int pr = 0; pr < 2; ++pr) {
          const int i = 4 * hf + 2 * pr;
          const f32x4 a = acc[jj][i] - st[i] * cs[jj], c = acc[jj][i + 1] - st[i + 1] * cs[jj];
          float hv4[4];
#pragma unroll
          for (int e = 0; e < 4; ++e) {
            const auto sw = __builtin_amdgcn_permlane16_swap(u_of(a[e]), u_of(c[e]), false, false);
            hv4[e] = __builtin_elementwise_maximum(f_of(sw[0]), 0.f) * f_of(sw[1]);
          }
          X[pr][0] = unmark(pk_bf16(hv4[0], hv4[1]));
          X[pr][1] = unmark(pk_bf16(hv4[2], hv4[3]));
        }
        const auto s0 = __builtin_amdgcn_permlane32_swap(X[0][0], X[1][0], false, false);
        const auto s1 = __builtin_amdgcn_permlane32_swap(X[0][1], X[1][1], false, false);
        st_dev(rs, vo, WS_HH + (nb * 12 + w * 3 + jj) * 256, (u32x4){s0[0], s1[0], s0[1], s1[1]});
        SCHED();
      }
    }
    STAMP(2, 5);
    asm volatile("s_waitcnt vmcnt(0)" ::: "memory");
  } else {
    if (conv) {
      u32x4 pc[6];
#pragma unroll
      for (int lc = 0; lc < 3; ++lc) {
        const int ch = min(3 * b + lc, D - 1);
        pc[2 * lc] = ld_pl(rs, lane * 16, WS_UT + ch * 4096 + hw * 1024);
        pc[2 * lc + 1] = *(const u32x4*)(ssmk + (size_t)ch * S + hw * 512 + lane * 8);
      }
      const unsigned uo = sb + CH_U + ((lane >> 1) & 3) * 1152 + 128 + ((lane >> 3) * 2 + (lane & 1)) * 16 + hw * 256;
      const unsigned ko = sb + CH_K + 32 + hw * 1024 + lane * 16;
#pragma unroll
      for (int lc = 0; lc < 3; ++lc) {
        lds_w128(uo + lc * CH_BYTES, pc[2 * lc]);
        lds_w128(ko + lc * CH_BYTES, pc[2 * lc + 1]);
      }
    }
    LGKM0_BAR();
    STAMP(1, 1);
#pragma unroll
    for (int i = 0; i < 6; ++i)
      if (!(DIAG & 512)) st_dev(rs, lane * 16, WS_HH + (tb * 8 + 2 * hw + i / 3) * 49152 + nb * 3072 + (i % 3) * 1024, (u32x4){MARK, MARK, MARK, MARK});
    const u16* wsrc = (hw >> 1) ? Wu : Wg;
    unsigned wvo[3];
#pragma unroll
    for (int i = 0; i < 3; ++i) {
      const int un = i * 64 + lane, blk = un / 48, ph = un - 48 * blk;
      const int lg = (ph + 48 - (blk & 1)) % 48, rw = lg / 12, cu = lg - 12 * rw;
      wvo[i] = ((16 * (hw & 1) + 4 * blk + rw) * F + 96 * nb + 8 * cu) * 2;
    }
    auto Wdma = [&](int ks) {
      const int slot = ks & 7;
      const unsigned dst = sb + (slot < 7 ? L_WA + slot * WSLOT : L_WA7) + (hw >> 1) * WUP + (hw & 1) * 3072;
      glds16_s3(wsrc + (size_t)ks * 32 * F, wvo[0], wvo[1] - 1024, wvo[2] - 2048, dst);
    };
#pragma unroll
    for (int s = 0; s < 7; ++s) Wdma(s);
    asm volatile("s_waitcnt vmcnt(21)" ::: "memory");
    {
      unsigned old = 0;
      if (lane == 0) old = __hip_atomic_fetch_add(cnt + 2, 1u, __ATOMIC_RELAXED, __HIP_MEMORY_SCOPE_WORKGROUP);
      if (__builtin_amdgcn_readfirstlane(old) == 3u && lane == 0)
        __hip_atomic_fetch_add((__attribute__((address_space(1))) unsigned*)(ws + WS_CNT + (8 + tb) * 256), 1u, __ATOMIC_RELAXED, __HIP_MEMORY_SCOPE_AGENT);
    }
    u32x4 wp[WREG + 1][3];
#pragma unroll
    for (int s = 0; s <= WREG; ++s)
#pragma unroll
      for (int i = 0; i < 3; ++i) wp[s][i] = *(const u32x4*)((const char*)(wsrc + (size_t)(7 + s) * 32 * F) + wvo[i]);
    SCHED();
    asm volatile("s_waitcnt vmcnt(%0)" :: "n"(3 * (WREG + 1)) : "memory");
    auto Wput = [&](int ks) {
      const int slot = ks & 7;
      const unsigned dst = sb + (slot < 7 ? L_WA + slot * WSLOT : L_WA7) + (hw >> 1) * WUP + (hw & 1) * 3072 + lane * 16;
#pragma unroll
      for (int i = 0; i < 3; ++i) lds_w128(dst + i * 1024, wp[ks - 7][i]);
    };
    STAMP(1, 4);
    RAW_BAR();
    Wput(7);
    RAW_BAR();
    const unsigned goff = g * 4096 + 256 * tb + 16 * t16;
    u32x4 hv[4][4];
    auto Hld = [&](int c, u32x4 (&d)[4]) {
#pragma unroll
      for (int i = 0; i < 4; ++i) d[i] = ld_dev(rs, goff, WS_HT + (64 * c + 16 * hw + 4 * i) * 4096);
      SCHED();
    };
    const unsigned auxo = (lane & 1) ? (8 + tb) * 256 : xcd * 256, auxw = (lane & 1) ? 16 : 256 - NCONV;
    unsigned aux = __builtin_amdgcn_raw_buffer_load_b32(rs, auxo, WS_CNT, 16);
    SCHED();
    Hld(0, hv[0]); Hld(1, hv[1]);
#pragma unroll
    for (int s = 8; s < 8 + WREG; ++s) Wput(s);
    LGKM0_BAR();
    SETTLE(max(max16(hv[0]), max16(hv[1])) == MARK, { Hld(0, hv[0]); Hld(1, hv[1]); });
    Hld(2, hv[2]); Hld(3, hv[3]);
    __builtin_amdgcn_s_setprio(P2PRIO);
    STAMP(2, 0);
    const unsigned lo0 = sb + (16 * hw + g) * 256 + ((t16 ^ (2 * g)) * 16);
    const unsigned lo1 = sb + (16 * hw + 4 + g) * 256 + ((t16 ^ (2 * (4 + g))) * 16);
    const unsigned sqo = sb + (16 * hw + (lane >> 2)) * 256 + ((nb ^ (2 * ((lane >> 2) & 7))) * 16) + p * 4;
    float sq0 = 0.f, sq1 = 0.f;
    unsigned sqv = 0;
    u32x4 dn = {0, 0, 0, 0};
    static_for<0, 10>([&](auto CC) {
      constexpr int c = CC.v;
      u32x4(&hc)[4] = hv[c & 3];
      constexpr int lo[11] = WDSCHED;
      constexpr int t0 = lo[c] < NWH ? lo[c] : NWH, t1 = lo[c + 1] < NWH ? lo[c + 1] : NWH;
      constexpr int p0 = c ? (lo[c ? c - 1 : 0] < NWH ? lo[c ? c - 1 : 0] : NWH) : 0;
      TR(5 * c);
      if (c >= 6) {
        constexpr int younger = 5 * (t0 - p0) + (c - 1 + 4 < 10 ? 4 : 0);
        asm volatile("s_waitcnt vmcnt(%0)" :: "n"(younger) : "memory");
      }
      if (c >= 2) SETTLE(max16(hc) == MARK, Hld(c, hc));
      TR(5 * c + 1);
      if (c >= 3) {
        if (__builtin_amdgcn_readfirstlane(min(min(dn[0], dn[1]), min(dn[2], dn[3]))) < (unsigned)(c - 2)) {
          int sp = 0;
#pragma clang loop unroll(disable)
          do {
            __builtin_amdgcn_s_sleep(1);
            dn = *(volatile LDS_AS const u32x4*)(uintptr_t)(sb + L_DONE);
          } while (__builtin_amdgcn_readfirstlane(min(min(dn[0], dn[1]), min(dn[2], dn[3]))) < (unsigned)(c - 2) && ++sp < (1 << 22));
        }
        CBAR();
        SCHED();
      }
      auto wdld = [&](int n) {
        const int t = t0 + n / 5, jj = n % 5;
        if (t < t1 && !(DIAG & 32)) {
          if (t < NH) wh[t < NH ? NH - 1 - t : 0][jj] = ld_dev(rs, lane * 16, wdoff + ((11 - t) * 5 + jj) * 1024);
          else wx[t < NH ? 0 : t - NH][jj] = ld_dev(rs, lane * 16, wdoff + ((11 - t) * 5 + jj) * 1024);
          SCHED();
        }
      };
      TR(5 * c + 2);
#pragma unroll
      for (int i = 0; i < 4; ++i) if (!(DIAG & 2)) lds_w128(((i & 1) ? lo1 : lo0) + (c % 3) * 16384 + (i >> 1) * 2048, hc[i]);
      CBAR();
      if (lane == 0) __hip_atomic_fetch_add(cnt + 4 + c, 1u, __ATOMIC_RELAXED, __HIP_MEMORY_SCOPE_WORKGROUP);
      CBAR();
      SCHED();
      TR(5 * c + 3);
      if (!(DIAG & 4)) {
      if (c >= 5 && c <= 8) { Wdma(2 * c + 2); Wdma(2 * c + 3); }
      }
      {
        const float lo = bf_lo(sqv), hi = bf_hi(sqv);
        sq0 = fmaf(lo, lo, sq0);
        sq1 = fmaf(hi, hi, sq1);
        asm volatile("" : "+v"(sq0), "+v"(sq1));
        sqv = *(LDS_AS const unsigned*)(uintptr_t)(sqo + (c % 3) * 16384);
      }
      {
        const int pi = 4 * c + hw - 5 * db;
        if (pi >= 0 && pi < 5 && (t16 >> 3) == half) {
#pragma unroll
          for (int i = 0; i < 4; ++i) {
            const int row = 4 * i + g;
            lds_w128(sb + L_RES + (16 * pi + row) * 128 + (((t16 & 7) ^ (((row >> 1) & 3) << 1)) * 16), hc[i]);
          }
        }
      }
      SCHED();
      if (c + 4 < 10 && !(DIAG & 8)) Hld(c + 4, hc);
      if (c == 0) {
        SETTLE(aux < auxw, aux = __builtin_amdgcn_raw_buffer_load_b32(rs, auxo, WS_CNT, 16));
        CBAR();
      }
#pragma unroll
      for (int n = 0; n < 10; ++n) wdld(n);
      if (c >= 2 && c < 9) { dn = *(volatile LDS_AS const u32x4*)(uintptr_t)(sb + L_DONE); SCHED(); }
    });
    sq0 = fmaf(bf_lo(sqv), bf_lo(sqv), sq0);
    sq1 = fmaf(bf_hi(sqv), bf_hi(sqv), sq1);
    {
      auto r = __builtin_amdgcn_permlane32_swap(u_of(sq0), u_of(sq1), false, false);
      float t = f_of(r[0]) + f_of(r[1]);
      auto r2 = __builtin_amdgcn_permlane16_swap(u_of(t), u_of(t), false, false);
      t = f_of(r2[0]) + f_of(r2[1]);
      t += dpp_f<0x124>(t);
      t += dpp_f<0x128>(t);
      *(LDS_AS float*)(uintptr_t)(sb + L_SQ + hw * 32 + (2 * p + (lane >> 5)) * 4) = t;
      asm volatile("s_waitcnt lgkmcnt(0)" ::: "memory");
      unsigned old = 0;
      if (lane == 0) old = __hip_atomic_fetch_add(cnt + 1, 1u, __ATOMIC_RELAXED, __HIP_MEMORY_SCOPE_WORKGROUP);
      if (__builtin_amdgcn_readfirstlane(old) == 3u) {
        CBAR();
        const unsigned a = sb + L_SQ + (lane & 7) * 4;
        const float v = (*(LDS_AS const float*)(uintptr_t)a + *(LDS_AS const float*)(uintptr_t)(a + 32)) +
                        (*(LDS_AS const float*)(uintptr_t)(a + 64) + *(LDS_AS const float*)(uintptr_t)(a + 96));
        __builtin_amdgcn_raw_buffer_store_b32(unmark(u_of(v)), rs, lane < 8 ? lane * 4 : 0x80000000u, WS_SQ + (128 * tb + 8 * nb) * 4, 16);
      }
    }
    __builtin_amdgcn_s_setprio(0);
    STAMP(2, 3);
    LGKM0_BAR();
#pragma unroll
    for (int st = 0; st < 12 - NWH; ++st)
#pragma unroll
      for (int jj = 0; jj < 5; ++jj)
        glds16_asm(ws + wdoff + (st * 5 + jj) * 1024 + lane * 16, sb + (st < 4 ? L_WA + st * 20480 : (st - 4) * 20480) + hw * 5120 + jj * 1024);
#pragma unroll
    for (int t = NWH - 1; t >= NH; --t) {
      const int st = 11 - t;
#pragma unroll
      for (int jj = 0; jj < 5; ++jj) lds_w128(sb + (st < 4 ? L_WA + st * 20480 : (st - 4) * 20480) + hw * 5120 + jj * 1024 + lane * 16, wx[t - NH][jj]);
    }
    asm volatile("s_waitcnt vmcnt(0) lgkmcnt(0)" ::: "memory");
    STAMP(2, 5);
  }
  RAW_BAR();

  {
    int lz = threadIdx.x;
    asm volatile("" : "+v"(lz));
    const int lane = lz & 63, g = lane >> 4, q = (lane >> 2) & 3, p = lane & 3, t16 = lane & 15;
    const unsigned hoff = WS_HH + ((tb * 8 + half * 4) * 192 + 48 * kg) * 256;
    auto loadS = [&](int s, u32x4 (&d)[4]) {
#pragma unroll
      for (int i = 0; i < 4; ++i) d[i] = ld_dev(rs, lane * 16, hoff + i * 49152 + s * 1024);
      SCHED();
    };
    f32x4 a3[4][5];
    if (isC) {
      constexpr int NS = 12 - NH;
      unsigned sv = __builtin_amdgcn_raw_buffer_load_b32(rs, t16 * 4, WS_SQ + (128 * tb + 64 * half + 16 * w) * 4, 16);
      SCHED();
      u32x4 hf[NS][4];
#pragma unroll
      for (int s = 0; s < NS; ++s) loadS(s, hf[s]);
      const unsigned wlb = sb + kg * 5120 + lane * 16;
      auto wreg = [&](int s) -> unsigned { return s < 4 ? L_WA + s * 20480 : (s - 4) * 20480; };
      u32x4 wc[2][5];
#pragma unroll
      for (int jj = 0; jj < 5; ++jj) wc[0][jj] = lds_r128(wlb + wreg(0) + jj * 1024);
      SETTLE(sv == MARK, sv = __builtin_amdgcn_raw_buffer_load_b32(rs, t16 * 4, WS_SQ + (128 * tb + 64 * half + 16 * w) * 4, 16));
      float rr2;
      {
        const float mean = *(LDS_AS const float*)(uintptr_t)(sb + L_ST + (64 * half + 16 * w + t16) * 4);
        float var = fmaf(-mean, mean, f_of(sv) * (1.0f / D));
        var = rnd_bf16(fmaxf(var, 0.f));
        const float sd = rnd_bf16(sqrtf(rnd_bf16(var + EPS)));
        rr2 = 1.0f / (sd * sd);
        asm volatile("" : "+v"(rr2));
      }
      SCHED();
      static_for<0, NS>([&](auto SS) {
        constexpr int s = SS.v;
        SETTLE(max16(hf[s]) == MARK, loadS(s, hf[s]));
        SCHED();
        if (s == 0) STAMP(3, 0);
        if (s == 4) { *(volatile LDS_AS unsigned*)(uintptr_t)(sb + L_DONE + w * 4) = 100u; SCHED(); }
#pragma unroll
        for (int jj = 0; jj < 5; ++jj) {
#pragma unroll
          for (int i = 0; i < 4; ++i) a3[i][jj] = mfma16(as_bf(wc[s & 1][jj]), as_bf(hf[s][(i + 0) & 3]), s == 0 ? (f32x4){0, 0, 0, 0} : a3[i][jj]);
          SCHED();
          if (s + 1 < NS) { wc[(s + 1) & 1][jj] = lds_r128(wlb + wreg(s + 1) + jj * 1024); SCHED(); }
        }
      });
      STAMP(3, 1);
      RAW_BAR();
#pragma unroll
      for (int i = 0; i < 4; ++i)
#pragma unroll
        for (int jj = 0; jj < 5; ++jj) a3[i][jj] += lds_r128f(sb + L_P3 + (kg * 20 + i * 5 + jj) * 1024 + lane * 16);
      static_for<0, 4>([&](auto II) {
        constexpr int i = II.v;
        if (i != w) {
          const int slot = (w - i - 1) & 3;
#pragma unroll
          for (int jj = 0; jj < 5; ++jj) lds_w128f(sb + (i * 15 + slot * 5 + jj) * 1024 + lane * 16, a3[i][jj]);
        }
      });
      f32x4 own[5];
#pragma unroll
      for (int jj = 0; jj < 5; ++jj) own[jj] = w == 0 ? a3[0][jj] : w == 1 ? a3[1][jj] : w == 2 ? a3[2][jj] : a3[3][jj];
      LGKM0_BAR();
      STAMP(3, 2);
#pragma unroll
      for (int jj = 0; jj < 5; ++jj) {
        const unsigned a = sb + (w * 15 + jj) * 1024 + lane * 16;
        const f32x4 o = ((own[jj] + lds_r128f(a)) + (lds_r128f(a + 5120) + lds_r128f(a + 10240))) * rr2;
        const int row = 16 * jj + 4 * g + q;
        const s16x4 hr = tr_read(sb + L_RES + row * 128 + ((w ^ ((row >> 1) & 3)) * 32) + p * 8);
        const u32x2 hu = __builtin_bit_cast(u32x2, hr);
        const unsigned o01 = pk_bf16(o[0], o[1]), o23 = pk_bf16(o[2], o[3]);
        u32x2 ov = {pk_bf16(bf_lo(hu[0]) + bf_lo(o01), bf_hi(hu[0]) + bf_hi(o01)),
                    pk_bf16(bf_lo(hu[1]) + bf_lo(o23), bf_hi(hu[1]) + bf_hi(o23))};
        *(u32x2*)(out + (size_t)(128 * tb + 64 * half + 16 * w + t16) * D + 80 * db + 16 * jj + 4 * g) = ov;
      }
    } else {
      constexpr int HW = 3;
      u32x4 hf[HW][4];
#pragma unroll
      for (int s = 0; s < HW; ++s) loadS(12 - NH + s, hf[s]);
      static_for<0, NH>([&](auto SS) {
        constexpr int s = SS.v;
        SETTLE(max16(hf[s % HW]) == MARK, loadS(12 - NH + s, hf[s % HW]));
        SCHED();
        if (s == 0) STAMP(3, 0);
#pragma unroll
        for (int jj = 0; jj < 5; ++jj)
#pragma unroll
          for (int i = 0; i < 4; ++i) a3[i][jj] = mfma16(as_bf(wh[s][jj]), as_bf(hf[s % HW][i]), s == 0 ? (f32x4){0, 0, 0, 0} : a3[i][jj]);
        SCHED();
        if (s + HW < NH) loadS(12 - NH + s + HW, hf[s % HW]);
      });
      STAMP(3, 1);
      {
        int sp = 0;
#pragma clang loop unroll(disable)
        while (++sp < (1 << 22)) {
          const u32x4 f = *(volatile LDS_AS const u32x4*)(uintptr_t)(sb + L_DONE);
          if (__builtin_amdgcn_readfirstlane(min(min(f[0], f[1]), min(f[2], f[3]))) >= 100u) break;
          __builtin_amdgcn_s_sleep(1);
        }
        CBAR();
      }
#pragma unroll
      for (int i = 0; i < 4; ++i)
#pragma unroll
        for (int jj = 0; jj < 5; ++jj) lds_w128f(sb + L_P3 + (kg * 20 + i * 5 + jj) * 1024 + lane * 16, a3[i][jj]);
      LGKM0_BAR();
      RAW_BAR();
      STAMP(3, 2);
    }
    STAMP(3, 3);
  }
  asm volatile("kf_code_end:");
}

extern "C" void kernel_launch(void* const* d_in, const int* in_sizes, int n_in, void* d_out, int out_size,
                              void* d_ws, size_t ws_size, hipStream_t stream) {
  const u16* q = (const u16*)d_in[0];
  const u16* k = (const u16*)d_in[1];
  const u16* v = (const u16*)d_in[2];
  const u16* shk = (const u16*)d_in[3];
  const u16* ssmk = (const u16*)d_in[4];
  const u16* ssmD = (const u16*)d_in[5];
  const u16* Wg = (const u16*)d_in[7];
  const u16* Wu = (const u16*)d_in[8];
  const u16* Wd = (const u16*)d_in[9];
  char* ws = (char*)d_ws;
  k0_prep<<<K0GRID, 256, 0, stream>>>(q, k, v, shk, ws);
  kf_main<3><<<256, 512, 0, stream>>>(ssmk, ssmD, Wg, Wu, Wd, ws, (u16*)d_out);
}
```

```cpp
#include <hip/hip_runtime.h>
#include <stdint.h>

typedef unsigned short u16;
typedef short s16x4 __attribute__((ext_vector_type(4)));
typedef short s16x8 __attribute__((ext_vector_type(8)));
typedef __bf16 bf16x8 __attribute__((ext_vector_type(8)));
typedef __bf16 bf16x2 __attribute__((ext_vector_type(2)));
typedef float f32x2 __attribute__((ext_vector_type(2)));
typedef float f32x4 __attribute__((ext_vector_type(4)));
typedef unsigned u32x2 __attribute__((ext_vector_type(2)));
typedef unsigned u32x4 __attribute__((ext_vector_type(4)));
#define LDS_AS __attribute__((address_space(3)))
#define DEVI __device__ __forceinline__

constexpr int S = 2048, D = 640, F = 1536;
constexpr float EPS = 1e-5f;
constexpr unsigned MARK = 0xffffffffu;
constexpr int SPIN_CAP = 1 << 14;

constexpr unsigned WS_UT = 0;
constexpr unsigned WS_XT = WS_UT + D * S * 2;
constexpr unsigned WS_HT = WS_XT + D * S * 2;
constexpr unsigned WS_ST = WS_HT + D * S * 2;
constexpr unsigned WS_CNT = WS_ST + S * 8;
constexpr unsigned WS_WD = WS_CNT + 8192;
constexpr unsigned WS_SQ = WS_WD + D * F * 2;
constexpr unsigned WS_HH = WS_SQ + S * 4;
constexpr unsigned WS_END = WS_HH + S * F * 2;

DEVI float bf_lo(unsigned u) { return __builtin_bit_cast(float, u << 16); }
DEVI float bf_hi(unsigned u) { return __builtin_bit_cast(float, u & 0xffff0000u); }
DEVI unsigned pk_bf16(float a, float b) {
  f32x2 v = {a, b};
  bf16x2 r = __builtin_convertvector(v, bf16x2);
  return __builtin_bit_cast(unsigned, r);
}
DEVI float rnd_bf16(float a) { return bf_lo(pk_bf16(a, a)); }
DEVI unsigned unmark(unsigned u) { return u < 0xfffffffeu ? u : 0xfffffffeu; }
DEVI s16x4 tr_read(unsigned lds_byte) {
  return __builtin_amdgcn_ds_read_tr16_b64_v4i16((LDS_AS s16x4*)(uintptr_t)lds_byte);
}
DEVI s16x4 lds_r64(unsigned a) { return *(volatile LDS_AS const s16x4*)(uintptr_t)a; }
DEVI u32x4 lds_r128(unsigned a) { return *(LDS_AS const u32x4*)(uintptr_t)a; }
DEVI void lds_w128(unsigned a, u32x4 v) { *(LDS_AS u32x4*)(uintptr_t)a = v; }
DEVI f32x4 lds_r128f(unsigned a) { return *(LDS_AS const f32x4*)(uintptr_t)a; }
DEVI void lds_w128f(unsigned a, f32x4 v) { *(LDS_AS f32x4*)(uintptr_t)a = v; }
DEVI bf16x8 cat8(s16x4 a, s16x4 b) {
  s16x8 ab = __builtin_shufflevector(a, b, 0, 1, 2, 3, 4, 5, 6, 7);
  return __builtin_bit_cast(bf16x8, ab);
}
DEVI bf16x8 as_bf(u32x4 v) { return __builtin_bit_cast(bf16x8, v); }
DEVI f32x4 mfma16(bf16x8 a, bf16x8 b, f32x4 c) { return __builtin_amdgcn_mfma_f32_16x16x32_bf16(a, b, c, 0, 0, 0); }
DEVI unsigned lds_addr(const void* p) { return (unsigned)(uintptr_t)(LDS_AS const char*)p; }
DEVI void glds16_asm(const void* gsrc, unsigned lds_dst) {
  unsigned keep;
  asm volatile("s_mov_b32 %0, m0\n\ts_mov_b32 m0, %2\n\ts_nop 0\n\tglobal_load_lds_dwordx4 %1, off sc1\n\ts_mov_b32 m0, %0"
               : "=&s"(keep) : "v"(gsrc), "s"(lds_dst) : "memory");
}
DEVI void glds16_s(const void* sbase, unsigned voff, unsigned lds_dst) {
  unsigned keep;
  asm volatile("s_mov_b32 %0, m0\n\ts_mov_b32 m0, %3\n\ts_nop 0\n\tglobal_load_lds_dwordx4 %1, %2\n\ts_mov_b32 m0, %0"
               : "=&s"(keep) : "v"(voff), "s"(sbase), "s"(lds_dst) : "memory");
}
DEVI void glds16_s3(const void* sbase, unsigned v0, unsigned v1, unsigned v2, unsigned lds_dst) {
  unsigned keep;
  asm volatile("s_mov_b32 %0, m0\n\ts_mov_b32 m0, %5\n\ts_nop 0\n\tglobal_load_lds_dwordx4 %1, %4\n\t"
               "global_load_lds_dwordx4 %2, %4 offset:1024\n\tglobal_load_lds_dwordx4 %3, %4 offset:2048\n\ts_mov_b32 m0, %0"
               : "=&s"(keep) : "v"(v0), "v"(v1), "v"(v2), "s"(sbase), "s"(lds_dst) : "memory");
}
typedef __amdgpu_buffer_rsrc_t rsrc_t;
DEVI u32x4 ld_dev(rsrc_t rs, unsigned voff, unsigned soff) { return __builtin_amdgcn_raw_buffer_load_b128(rs, voff, soff, 16); }
DEVI u32x4 ld_pl(rsrc_t rs, unsigned voff, unsigned soff) { return __builtin_amdgcn_raw_buffer_load_b128(rs, voff, soff, 0); }
DEVI void st_dev(rsrc_t rs, unsigned voff, unsigned soff, u32x4 v) {
  __builtin_amdgcn_raw_buffer_store_b128(v, rs, voff, soff, 16);
  asm volatile("s_nop 1" :: "v"(v) : "memory");
}
DEVI unsigned max4(u32x4 v) { return max(max(v[0], v[1]), max(v[2], v[3])); }
DEVI unsigned max16(const u32x4 (&v)[4]) { return max(max(max4(v[0]), max4(v[1])), max(max4(v[2]), max4(v[3]))); }
template <int CTRL> DEVI float dpp_f(float x) {
  return __builtin_bit_cast(float, __builtin_amdgcn_mov_dpp(__builtin_bit_cast(int, x), CTRL, 0xf, 0xf, true));
}
template <int I> struct IC { static constexpr int v = I; };
template <int I, int N, class Fn> DEVI void static_for(Fn&& f) {
  if constexpr (I < N) { f(IC<I>{}); static_for<I + 1, N>(f); }
}
DEVI float f_of(unsigned u) { return __builtin_bit_cast(float, u); }
DEVI unsigned u_of(float f) { return __builtin_bit_cast(unsigned, f); }

#define STAMP(k, i) do {} while (0)
#define TR(i) do {} while (0)
#define CBAR() asm volatile("" ::: "memory")
#define SCHED() __builtin_amdgcn_sched_barrier(0)
#define RAW_BAR() do { CBAR(); __builtin_amdgcn_s_barrier(); CBAR(); SCHED(); } while (0)
#define LGKM0_BAR() do { asm volatile("s_waitcnt lgkmcnt(0)" ::: "memory"); __builtin_amdgcn_s_barrier(); CBAR(); SCHED(); } while (0)
#define SETTLE(BAD, RELOAD) do { if (__builtin_expect(__any(BAD), 0)) { int sp_ = 0; \
    _Pragma("clang loop unroll(disable)") do { __builtin_amdgcn_s_sleep(2); RELOAD; } while (__any(BAD) && ++sp_ < SPIN_CAP); } } while (0)

#define K0AUX 0
#define NOPOISON 0
#define K0GRID (K0_TR + K0_ST)
__global__ void k_tail(char* ws) { if (blockIdx.x == 9999) ws[0] = 1; }
constexpr int K0_TR = 320, K0_ST = 128;
__global__ __launch_bounds__(256) void k0_prep(const u16* __restrict__ x, const u16* __restrict__ kin,
                                               const u16* __restrict__ vin, const u16* __restrict__ shk,
                                               const u16* __restrict__ ssmk, char* __restrict__ ws) {
  __shared__ __attribute__((aligned(16))) char smem[2 * 64 * 144];
  const int tid = threadIdx.x, lane = tid & 63;
  const int w = __builtin_amdgcn_readfirstlane(tid >> 6);
  const int b = blockIdx.x;
  const int g = lane >> 4, q = (lane >> 2) & 3, p = lane & 3;
  const rsrc_t rs0 = __builtin_amdgcn_make_buffer_rsrc(ws, 0, WS_END, 0x00020000);
#define K0ST(off, v) __builtin_amdgcn_raw_buffer_store_b128(v, rs0, off, 0, K0AUX)
  STAMP(0, 0);
  if (b < K0_TR) {
    const int tt = b & 31, cg = b >> 5;
    const int r2 = tid >> 3, cc = tid & 7;
    const int t0 = 64 * tt + 2 * r2, c0 = 64 * cg + 8 * cc;
    u32x4 kr[5], vr[2], xr[2], sw[4];
#pragma unroll
    for (int i = 0; i < 5; ++i) {
      const int t = t0 - 3 + i;
      const int tc = t < 0 ? 0 : t;
      kr[i] = *(const u32x4*)(kin + (size_t)tc * D + c0);
      if (t < 0) kr[i] = (u32x4){0, 0, 0, 0};
    }
#pragma unroll
    for (int i = 0; i < 2; ++i) {
      vr[i] = *(const u32x4*)(vin + (size_t)(t0 + i) * D + c0);
      xr[i] = *(const u32x4*)(x + (size_t)(t0 + i) * D + c0);
    }
#pragma unroll
    for (int i = 0; i < 4; ++i) sw[i] = *(const u32x4*)(shk + (size_t)c0 * 4 + 8 * i);
#pragma unroll
    for (int i = 0; i < 2; ++i) if (!NOPOISON) K0ST(WS_HT + b * 8192 + i * 4096 + tid * 16, ((u32x4){MARK, MARK, MARK, MARK}));
    char* su = smem;
    char* sx = smem + 64 * 144;
#pragma unroll
    for (int i = 0; i < 2; ++i) {
      u32x4 o;
#pragma unroll
      for (int cp = 0; cp < 4; ++cp) {
        float r[2];
#pragma unroll
        for (int hh = 0; hh < 2; ++hh) {
          const unsigned w01 = sw[cp][2 * hh], w23 = sw[cp][2 * hh + 1];
          float kk[4];
#pragma unroll
          for (int j = 0; j < 4; ++j) kk[j] = hh ? bf_hi(kr[i + 3 - j][cp]) : bf_lo(kr[i + 3 - j][cp]);
          float a = kk[3] * bf_hi(w23);
          a = fmaf(kk[2], bf_lo(w23), a);
          a = fmaf(kk[1], bf_hi(w01), a);
          a = fmaf(kk[0], bf_lo(w01), a);
          r[hh] = a;
        }
        const unsigned ks = pk_bf16(r[0], r[1]);
        o[cp] = pk_bf16(bf_lo(ks) * bf_lo(vr[i][cp]), bf_hi(ks) * bf_hi(vr[i][cp]));
      }
      *(u32x4*)(su + (2 * r2 + i) * 144 + cc * 16) = o;
      *(u32x4*)(sx + (2 * r2 + i) * 144 + cc * 16) = xr[i];
    }
    STAMP(0, 1);
    __syncthreads();
    STAMP(0, 2);
    const int c = 64 * cg + 16 * w + (lane & 15);
    const unsigned colb = (16 * w + 4 * p) * 2;
#pragma unroll
    for (int si = 0; si < 2; ++si) {
      const int ci = 4 * si + g;
      {
        const int rho = ci >> 1, half = ci & 1;
        const int row = 4 * (8 * half + q) + rho;
        const unsigned a = lds_addr(su) + row * 144 + colb;
        const bf16x8 v = cat8(tr_read(a), tr_read(a + 16 * 144));
        K0ST(WS_UT + ((c * 32 + tt) * 64 + ci * 8) * 2, __builtin_bit_cast(u32x4, v));
      }
      {
        const int row = 8 * ci + q;
        const unsigned a = lds_addr(sx) + row * 144 + colb;
        const bf16x8 v = cat8(tr_read(a), tr_read(a + 4 * 144));
        K0ST(WS_XT + (c * S + 64 * tt + ci * 8) * 2, __builtin_bit_cast(u32x4, v));
      }
    }
  } else {
    const int row = (b - K0_TR) * 16 + 4 * w + (lane >> 4), sub = lane & 15;
    if (b == K0_TR && tid < 24) *(unsigned*)(ws + WS_CNT + tid * 256) = 0u;
    unsigned pf0;
    {
      unsigned long long pc;
      int off;
      unsigned len;
      asm volatile("s_getpc_b64 %0\n\tk0_pc_here:\n\ts_mov_b32 %1, kf_code_begin - k0_pc_here\n\ts_mov_b32 %2, kf_code_end - kf_code_begin"
                   : "=s"(pc), "=s"(off), "=s"(len));
      const unsigned o = ((b - K0_TR) >> 3) * 4096 + tid * 16;
      pf0 = *(const __attribute__((address_space(1))) unsigned*)(pc + off + (o + 64 < len ? o : 0));
    }
    u32x4 xr[5];
#pragma unroll
    for (int i = 0; i < 5; ++i) xr[i] = *(const u32x4*)(x + (size_t)row * D + (sub + 16 * i) * 8);
    float s = 0.f;
#pragma unroll
    for (int i = 0; i < 5; ++i)
#pragma unroll
      for (int j = 0; j < 4; ++j) s += bf_lo(xr[i][j]) + bf_hi(xr[i][j]);
    s += dpp_f<0xB1>(s); s += dpp_f<0x4E>(s); s += dpp_f<0x124>(s); s += dpp_f<0x128>(s);
    const float mean = s * (1.0f / D);
    float qq = 0.f;
#pragma unroll
    for (int i = 0; i < 5; ++i)
#pragma unroll
      for (int j = 0; j < 4; ++j) {
        const float a = bf_lo(xr[i][j]) - mean, c = bf_hi(xr[i][j]) - mean;
        qq = fmaf(a, a, qq);
        qq = fmaf(c, c, qq);
      }
    qq += dpp_f<0xB1>(qq); qq += dpp_f<0x4E>(qq); qq += dpp_f<0x124>(qq); qq += dpp_f<0x128>(qq);
    const float var = rnd_bf16(qq * (1.0f / D));
    const float sd = rnd_bf16(sqrtf(rnd_bf16(var + EPS)));
    if (sub == 0) {
      f32x2 st = {rnd_bf16(mean), 1.0f / sd};
      *(f32x2*)(ws + WS_ST + (size_t)row * 8) = st;
      *(unsigned*)(ws + WS_SQ + (size_t)row * 4) = MARK;
    }
    asm volatile("" :: "v"(pf0));
  }
  STAMP(0, 3);
}

#define STATS_MODE 1
constexpr int NCONV = 214;
constexpr int CH_U = 0, CH_K = 4608, CH_Y = CH_K + 4128, CH_C = CH_Y + 8192, CH_BYTES = CH_C + 8192;
constexpr int WSLOT = 12320, WUP = 6176;
constexpr int L_WA7 = 49152, L_RES = 61504, L_ST = L_RES + 10240, L_CS = L_ST + 1024, L_SQ = L_CS + 768, L_CNT = 76000, L_RDY = L_CNT + 16, L_DONE = L_RDY + 48, L_WA = 76800, L_P3 = L_WA;
#define DIAG 0
#define WDSCHED {0, 1, 3, 5, 7, 7, 7, 8, 9, 10, 10}
#define P2PRIO 1
#define NH 5
#define PRE 4
#define NWH 10
constexpr int WREG = 4;

DEVI float addf(float a, float b) { float r = a + b; asm("" : "+v"(r)); return r; }
DEVI void conv_store(unsigned base, int ct, int a16, int g, const f32x4 (&z)[4]) {
  f32x4 y = z[0], c = z[3];
  y[1] = addf(y[1], z[1][0]);
  y[2] = addf(addf(y[2], z[1][1]), z[2][0]);
  y[3] = addf(addf(y[3], z[1][2]), addf(z[2][1], z[3][0]));
  c[1] = addf(addf(c[1], z[1][3]), z[2][2]);
  c[2] = addf(c[2], z[2][3]);
  const unsigned o = base + ct * 1024 + g * 256 + ((a16 ^ ((g >> 1) << 3)) * 16);
  lds_w128f(o + CH_Y, y);
  lds_w128f(o + CH_C, c);
}
template <int TOP, int NT, int EX, int NCH>
DEVI void conv_run(unsigned base0, int lane) {
  const int a16 = lane & 15, g = lane >> 4, q = (lane >> 2) & 3, pp = lane & 3;
  const unsigned ab = base0 + CH_U + 128 + 2 * (4 * a16 - 4 - 8 * g) - 64;
  const unsigned aa = base0 + CH_K + 32 + 2 * (32 * g - 4 * q + 4 * pp);
  constexpr int LOW = TOP - NT + 1, NS = 2 * TOP + 2, NA = 2 * TOP + 2;
  f32x4 acc[NT][4], acc0[4];
  bf16x8 A[2][NA], B[3][4];
  auto loadA = [&](int ch, int n) { A[ch & 1][n] = cat8(tr_read(aa + ch * CH_BYTES + 256 * n + 32), tr_read(aa + ch * CH_BYTES + 256 * n)); };
  auto loadStep = [&](int gs) {
    const int ch = gs / NS, st = gs % NS;
    if (st < 2) {
#pragma unroll
      for (int t = NT - 1; t >= 0; --t) loadA(ch, 2 * (LOW + t) + 1 - st);
    }
#pragma unroll
    for (int r = 0; r < 4; ++r) B[gs % 3][r] = cat8(lds_r64(ab + ch * CH_BYTES + 64 * st + r * 1152), lds_r64(ab + ch * CH_BYTES + 64 * st + r * 1152 + 8));
    if (st >= 2 && 2 * LOW + 1 - st > 2 * EX + 1) loadA(ch, 2 * LOW + 1 - st);
    if (2 * EX + 1 - st >= 0) loadA(ch, 2 * EX + 1 - st);
  };
  loadStep(0);
  loadStep(1);
  SCHED();
#pragma unroll
  for (int gs = 0; gs < NCH * NS; ++gs) {
    const int ch = gs / NS, st = gs % NS;
    if (gs + 2 < NCH * NS) loadStep(gs + 2);
#pragma unroll
    for (int t = NT - 1; t >= 0; --t) {
      const int n = 2 * (LOW + t) + 1 - st;
      if (n >= 0) {
#pragma unroll
        for (int r = 0; r < 4; ++r) acc[t][r] = mfma16(A[ch & 1][n], B[gs % 3][r], st == 0 ? (f32x4){0, 0, 0, 0} : acc[t][r]);
      }
    }
    if (2 * EX + 1 - st >= 0) {
#pragma unroll
      for (int r = 0; r < 4; ++r) acc0[r] = mfma16(A[ch & 1][2 * EX + 1 - st], B[gs % 3][r], st == 0 ? (f32x4){0, 0, 0, 0} : acc0[r]);
    }
    if (st == 2 * EX + 2) conv_store(base0 + ch * CH_BYTES, EX, a16, g, acc0);
    if (st >= 2 * LOW + 2 && !(st & 1)) conv_store(base0 + ch * CH_BYTES, (st - 2) / 2, a16, g, acc[(st - 2) / 2 - LOW]);
#pragma unroll
    for (int i = 0; i < 20; ++i) {
      __builtin_amdgcn_sched_group_barrier(0x8, 1, 0);
      __builtin_amdgcn_sched_group_barrier(0x100, 1, 0);
      __builtin_amdgcn_sched_group_barrier(0x2, 1, 0);
    }
    SCHED();
    if (st == NS - 1) { conv_store(base0 + ch * CH_BYTES, TOP, a16, g, acc[NT - 1]); SCHED(); }
  }
}

template <int PH>
__global__ __launch_bounds__(512, 2) void kf_main(const u16* __restrict__ ssmk, const u16* __restrict__ ssmD,
                                                  const u16* __restrict__ Wg, const u16* __restrict__ Wu,
                                                  const u16* __restrict__ Wd, char* __restrict__ ws,
                                                  u16* __restrict__ out) {
  __shared__ __attribute__((aligned(16))) char smem[163840];
  const int tid = threadIdx.x, lane = tid & 63;
  const int w = __builtin_amdgcn_readfirstlane(tid >> 6);
  const int b = blockIdx.x, xcd = b & 7, rr = b >> 3;
  const unsigned sb = lds_addr(smem);
  const rsrc_t rs = __builtin_amdgcn_make_buffer_rsrc(ws, 0, WS_END, 0x00020000);
  const int g = lane >> 4, q = (lane >> 2) & 3, p = lane & 3, t16 = lane & 15;
  const int tb = 4 * (xcd & 3) + (rr & 3), nb = 8 * (xcd >> 2) + (rr >> 2);
  const int half = nb & 1, db = nb >> 1;
  const bool isC = w < 4;
  const int hw = w & 3;
  const bool conv = b < NCONV;
  const int nch = conv ? min(3, D - 3 * b) : 0;
  LDS_AS unsigned* cnt = (LDS_AS unsigned*)(uintptr_t)(sb + L_CNT);
  STAMP(1, 0);
  unsigned pfacc = 0, pfacc2 = 0;
  if (w == 3) {
    unsigned len;
    asm volatile("kf_code_begin:\n\ts_mov_b32 %0, kf_code_end - kf_code_begin" : "=s"(len));
    const char* pc = (const char*)__builtin_amdgcn_s_getpc();
    const unsigned o = (rr * 2) * 1024 + lane * 16;
    const unsigned v0 = *(const __attribute__((address_space(1))) unsigned*)(pc + (o + 64 < len ? o : 0));
    const unsigned v1 = *(const __attribute__((address_space(1))) unsigned*)(pc + (o + 1088 < len ? o + 1024 : 0));
    pfacc = v0; pfacc2 = v1;
  }
  if (tid < 32) cnt[tid] = 0;

  if (!conv) {
    const int u = (b - NCONV) * 8 + w;
    if (u < 240) {
      const int ks2 = u / 5, dq = u % 5;
      constexpr int RS = 288;
      const unsigned sm = sb + w * 32 * RS;
      u32x4 v8[8];
#pragma unroll
      for (int i = 0; i < 8; ++i) v8[i] = *(const u32x4*)(Wd + (size_t)(32 * ks2 + 4 * i + g) * D + 128 * dq + 8 * t16);
#pragma unroll
      for (int i = 0; i < 8; ++i) lds_w128(sm + (4 * i + g) * RS + t16 * 16, v8[i]);
      CBAR();
#pragma unroll
      for (int t = 0; t < 8; ++t) {
        const int dt = 8 * dq + t;
        const unsigned a = sm + (8 * g + q) * RS + t * 32 + p * 8;
        const bf16x8 v = cat8(tr_read(a), tr_read(a + 4 * RS));
        st_dev(rs, lane * 16, WS_WD + (((dt / 5) * 48 + ks2) * 5 + (dt % 5)) * 1024, __builtin_bit_cast(u32x4, v));
      }
    }
    asm volatile("s_waitcnt vmcnt(0)" ::: "memory");
    __syncthreads();
    if (tid < 8) __hip_atomic_fetch_add((__attribute__((address_space(1))) unsigned*)(ws + WS_CNT + tid * 256), 1u, __ATOMIC_RELAXED, __HIP_MEMORY_SCOPE_AGENT);
  }

  f32x4 acc[3][8];
  const int kg = w & 3;
  const unsigned wdoff = WS_WD + (db * 48 + 12 * kg) * 5 * 1024;
  u32x4 wh[NH][5];
  u32x4 wx[5][5];

  if (isC) {
    u32x4 xv[3], st4[4];
    float dval[3];
    if (conv) {
#pragma unroll
      for (int i = 0; i < 3; ++i) {
        const int ch = min(3 * b + i, D - 1);
        xv[i] = ld_pl(rs, tid * 16, WS_XT + ch * 4096);
        dval[i] = bf_lo(ssmD[ch]);
      }
#pragma unroll
      for (int i = 0; i < 4; ++i) st4[i] = ld_pl(rs, tid * 64 + i * 16, WS_ST);
      if (w < 3) {
        const unsigned cb = sb + w * CH_BYTES;
        if (lane < 32) lds_w128(cb + CH_U + (lane >> 3) * 1152 + (lane & 7) * 16, (u32x4){0, 0, 0, 0});
        else if (lane < 34) lds_w128(cb + CH_K + (lane - 32) * 16, (u32x4){0, 0, 0, 0});
      }
    }
    LGKM0_BAR();
    asm volatile("" :: "v"(pfacc), "v"(pfacc2));
    STAMP(1, 1);
    if (conv) {
      if (w < 3) {
        conv_run<7, 4, 1, 1>(sb + w * CH_BYTES, lane);
      } else {
        conv_run<3, 2, 0, 3>(sb, lane);
      }
      STAMP(1, 2);
      asm volatile("s_waitcnt lgkmcnt(0)" ::: "memory");
      if (lane == 0) __hip_atomic_fetch_add(cnt, 1u, __ATOMIC_RELAXED, __HIP_MEMORY_SCOPE_WORKGROUP);
#pragma clang loop unroll(disable)
      for (int sp = 0; sp < (1 << 20); ++sp) {
        if (__builtin_amdgcn_readfirstlane(*(volatile LDS_AS unsigned*)cnt) >= 4u) break;
        __builtin_amdgcn_s_sleep(1);
      }
      CBAR();
      STAMP(1, 3);
      {
        auto pos = [&](int blk) -> unsigned { const int gg = blk & 3; return (blk >> 6) * 1024 + gg * 256 + ((((blk >> 2) & 15) ^ ((gg >> 1) << 3)) * 16); };
        const unsigned p0 = sb + pos(2 * tid), p1 = sb + pos(2 * tid + 1), pm = sb + pos(tid ? 2 * tid - 1 : 0);
        f32x4 y0[3], y1[3], c0[3], c1[3];
        unsigned uu[3][4];
#pragma unroll
        for (int lc = 0; lc < 3; ++lc) {
          y0[lc] = lds_r128f(p0 + lc * CH_BYTES + CH_Y);
          c0[lc] = lds_r128f(pm + lc * CH_BYTES + CH_C);
          y1[lc] = lds_r128f(p1 + lc * CH_BYTES + CH_Y);
          c1[lc] = lds_r128f(p0 + lc * CH_BYTES + CH_C);
#pragma unroll
          for (int r = 0; r < 4; ++r) uu[lc][r] = *(LDS_AS const unsigned*)(uintptr_t)(sb + lc * CH_BYTES + CH_U + r * 1152 + 128 + tid * 4);
        }
        SCHED();
#pragma unroll
        for (int lc = 0; lc < 3; ++lc) {
          float h[8];
#pragma unroll
          for (int e = 0; e < 8; ++e) {
            float y = e < 4 ? y0[lc][e & 3] : y1[lc][e & 3];
            if ((e & 3) < 3) y += e < 4 ? (tid ? c0[lc][(e & 3) + 1] : 0.f) : c1[lc][(e & 3) + 1];
            const float u = e < 4 ? bf_lo(uu[lc][e & 3]) : bf_hi(uu[lc][e & 3]);
            const float xx = (e & 1) ? bf_hi(xv[lc][e >> 1]) : bf_lo(xv[lc][e >> 1]);
            const float m = f_of(st4[e >> 1][2 * (e & 1)]), rsd = f_of(st4[e >> 1][2 * (e & 1) + 1]);
            h[e] = fmaf((xx - m) * rsd, fmaf(u, dval[lc], y), xx);
          }
          u32x4 o = {unmark(pk_bf16(h[0], h[1])), unmark(pk_bf16(h[2], h[3])), unmark(pk_bf16(h[4], h[5])), unmark(pk_bf16(h[6], h[7]))};
          st_dev(rs, lc < nch ? tid * 16 : 0x80000000u, WS_HT + min(3 * b + lc, D - 1) * 4096, o);
          SCHED();
        }
      }
    }
    STAMP(1, 4);
    LGKM0_BAR();
    unsigned wa[2][3];
    {
      const int r = 4 * g + q;
#pragma unroll
      for (int jj = 0; jj < 3; ++jj) {
        wa[0][jj] = sb + L_WA7 + (p & 1) * WUP + (r >> 2) * 768 + ((12 * (r & 3) + 3 * w + jj + ((r >> 2) & 1)) % 48) * 16 + (p >> 1) * 8;
        wa[1][jj] = wa[0][jj] + (L_WA + 3 * WSLOT - L_WA7);
        asm volatile("" : "+v"(wa[0][jj]), "+v"(wa[1][jj]));
      }
    }
    auto wadr = [&](int slot, int jj) -> unsigned {
      return slot == 7 ? wa[0][jj] : slot < 3 ? wa[0][jj] + (L_WA - L_WA7 + slot * WSLOT) : wa[1][jj] + (slot - 3) * WSLOT;
    };
    auto loadW = [&](int slot, bf16x8 (&dst)[3]) {
#pragma unroll
      for (int jj = 0; jj < 3; ++jj) dst[jj] = cat8(tr_read(wadr(slot, jj)), tr_read(wadr(slot, jj) + 3072));
    };
    bf16x8 wr[WREG][3], wl[3];
#pragma unroll
    for (int s = 0; s < WREG; ++s) loadW(s, wr[s]);
    LGKM0_BAR();
    u32x4 ones_u = {0x3f803f80u, 0x3f803f80u, 0x3f803f80u, 0x3f803f80u};
    asm volatile("" : "+v"(ones_u));
    const bf16x8 ones = as_bf(ones_u);
    f32x4 cs[3] = {{0, 0, 0, 0}, {0, 0, 0, 0}, {0, 0, 0, 0}};
#pragma unroll
    for (int s = 0; s < WREG; ++s)
#pragma unroll
      for (int jj = 0; jj < 3; ++jj) cs[jj] = mfma16(wr[s][jj], ones, cs[jj]);
    RAW_BAR();
#pragma unroll
    for (int s = WREG; s < 8 + WREG; ++s) {
      loadW(s & 7, wl);
#pragma unroll
      for (int jj = 0; jj < 3; ++jj) cs[jj] = mfma16(wl[jj], ones, cs[jj]);
    }
#pragma unroll
    for (int jj = 0; jj < 3; ++jj) {
      lds_w128f(sb + L_CS + w * 192 + jj * 64 + g * 16, cs[jj]);
      cs[jj] = (f32x4){0, 0, 0, 0};
    }
    SCHED();
    unsigned ta[8];
    {
      const unsigned lb = sb + (4 * g + q) * 256 + 32 * (4 * (g & 1) + q) + 16 * (p >> 1) + 8 * (p & 1);
#pragma unroll
      for (int i = 0; i < 8; ++i) ta[i] = lb ^ (32 * ((i + 2 * w) & 7));
    }
#pragma unroll
    for (int jj = 0; jj < 3; ++jj)
#pragma unroll
      for (int i = 0; i < 8; ++i) acc[jj][i] = (f32x4){0, 0, 0, 0};
    s16x4 af[3][4][2], wq[2][3][2];
    auto hoff = [&](int hs) -> unsigned { const int ks = hs >> 1; return ((ks >> 1) % 3) * 16384 + (ks & 1) * 8192; };
    f32x4 sacc[2] = {{0, 0, 0, 0}, {0, 0, 0, 0}};
    auto waitReady = [&](unsigned f, int c) {
      if (__builtin_expect(__builtin_amdgcn_readfirstlane(f) < 4u, 0)) {
        int sp = 0;
        TR(2 * c);
#pragma clang loop unroll(disable)
        do {
          __builtin_amdgcn_s_sleep(1);
          f = *(volatile LDS_AS const unsigned*)(uintptr_t)(sb + L_RDY + 4 * c);
        } while (__builtin_amdgcn_readfirstlane(f) < 4u && ++sp < (1 << 22));
        TR(2 * c + 1);
      }
      CBAR();
      SCHED();
    };
    unsigned rdy = 0;
    waitReady(rdy, 0);
    STAMP(2, 1);
#pragma unroll
    for (int i = 0; i < 8; ++i) { af[i >> 2][i & 3][0] = tr_read(ta[i]); af[i >> 2][i & 3][1] = tr_read(ta[i] + 4096); }
    SCHED();
    static_for<0, 40>([&](auto HS) {
      constexpr int hs = HS.v, ks = hs >> 1, cur = hs & 1, cb = hs % 3, nx = (hs + 2) % 3;
      if (hs + 2 < 40 && ((hs + 2) & 3) == 0) waitReady(rdy, (hs + 2) / 4);
      if ((hs & 3) == 1 && hs + 3 < 40) { rdy = *(volatile LDS_AS const unsigned*)(uintptr_t)(sb + L_RDY + 4 * ((hs + 3) / 4)); SCHED(); }
      if ((hs & 3) == 0 && hs >= 4 && hs <= 28) {
        *(volatile LDS_AS unsigned*)(uintptr_t)(sb + L_DONE + w * 4) = hs / 4;
        SCHED();
      }
      constexpr bool refill = !cur && ks + 1 >= WREG && ks + 1 < 20;
      constexpr int wo = (ks + 1) & 7;
      int gap = 0;
      auto filler = [&]() {
        const int n = gap++;
        if (n < 8) {
          if (hs + 2 < 40) af[nx][n >> 1][n & 1] = tr_read(ta[4 * cur + (n >> 1)] + hoff(hs + 2) + (n & 1) * 4096);
        } else if (refill && n < 14) {
          wq[(ks + 1) & 1][(n - 8) >> 1][n & 1] = tr_read(wadr(wo, (n - 8) >> 1) + (n & 1) * 3072);
        }
        SCHED();
      };
#pragma unroll
      for (int jj = 0; jj < 3; ++jj) {
        const bf16x8 wv = ks < WREG ? wr[ks < WREG ? ks : 0][jj] : cat8(wq[ks & 1][jj][0], wq[ks & 1][jj][1]);
#pragma unroll
        for (int i = 0; i < 4; ++i) {
          acc[jj][4 * cur + i] = mfma16(wv, cat8(af[cb][i][0], af[cb][i][1]), acc[jj][4 * cur + i]);
          SCHED();
          filler();
        }
        if (ks >= 8 + WREG && cur) { cs[jj] = mfma16(wv, ones, cs[jj]); SCHED(); }
        if (!cur && jj < 2) { sacc[jj] = mfma16(ones, cat8(af[cb][jj][0], af[cb][jj][1]), sacc[jj]); SCHED(); filler(); }
      }
    });
#pragma unroll
    for (int i = 0; i < 2; ++i) *(LDS_AS float*)(uintptr_t)(sb + L_ST + ((2 * w + i) * 16 + t16) * 4) = sacc[i][0] * (1.0f / D);
    STAMP(2, 2);
    LGKM0_BAR();
    STAMP(2, 4);
    int lz = threadIdx.x;
    asm volatile("" : "+v"(lz));
    const int lane = lz & 63, g = lane >> 4, t16 = lane & 15;
#pragma unroll
    for (int jj = 0; jj < 3; ++jj) cs[jj] += lds_r128f(sb + L_CS + w * 192 + jj * 64 + g * 16);
    float st[8];
#pragma unroll
    for (int i = 0; i < 8; ++i) st[i] = rnd_bf16(*(LDS_AS const float*)(uintptr_t)(sb + L_ST + (((i + 2 * w) & 7) * 16 + t16) * 4));
    SCHED();
#pragma unroll
    for (int hf = 0; hf < 2; ++hf) {
      const unsigned vo = (tb * 8 + ((4 * hf + 2 * (g >> 1) + (g & 1) + 2 * w) & 7)) * 49152 + t16 * 16;
#pragma unroll
      for (int jj = 0; jj < 3; ++jj) {
        unsigned X[2][2];
#pragma unroll
        for (int pr = 0; pr < 2; ++pr) {
          const int i = 4 * hf + 2 * pr;
          const f32x4 a = acc[jj][i] - st[i] * cs[jj], c = acc[jj][i + 1] - st[i + 1] * cs[jj];
          float hv4[4];
#pragma unroll
          for (int e = 0; e < 4; ++e) {
            const auto sw = __builtin_amdgcn_permlane16_swap(u_of(a[e]), u_of(c[e]), false, false);
            hv4[e] = __builtin_elementwise_maximum(f_of(sw[0]), 0.f) * f_of(sw[1]);
          }
          X[pr][0] = unmark(pk_bf16(hv4[0], hv4[1]));
          X[pr][1] = unmark(pk_bf16(hv4[2], hv4[3]));
        }
        const auto s0 = __builtin_amdgcn_permlane32_swap(X[0][0], X[1][0], false, false);
        const auto s1 = __builtin_amdgcn_permlane32_swap(X[0][1], X[1][1], false, false);
        st_dev(rs, vo, WS_HH + (nb * 12 + w * 3 + jj) * 256, (u32x4){s0[0], s1[0], s0[1], s1[1]});
        SCHED();
      }
    }
    STAMP(2, 5);
    asm volatile("s_waitcnt vmcnt(0)" ::: "memory");
  } else {
    if (conv) {
      u32x4 pc[6];
#pragma unroll
      for (int lc = 0; lc < 3; ++lc) {
        const int ch = min(3 * b + lc, D - 1);
        pc[2 * lc] = ld_pl(rs, lane * 16, WS_UT + ch * 4096 + hw * 1024);
        pc[2 * lc + 1] = *(const u32x4*)(ssmk + (size_t)ch * S + hw * 512 + lane * 8);
      }
      const unsigned uo = sb + CH_U + ((lane >> 1) & 3) * 1152 + 128 + ((lane >> 3) * 2 + (lane & 1)) * 16 + hw * 256;
      const unsigned ko = sb + CH_K + 32 + hw * 1024 + lane * 16;
#pragma unroll
      for (int lc = 0; lc < 3; ++lc) {
        lds_w128(uo + lc * CH_BYTES, pc[2 * lc]);
        lds_w128(ko + lc * CH_BYTES, pc[2 * lc + 1]);
      }
    }
    LGKM0_BAR();
    STAMP(1, 1);
#pragma unroll
    for (int i = 0; i < 6; ++i)
      if (!(DIAG & 512)) st_dev(rs, lane * 16, WS_HH + (tb * 8 + 2 * hw + i / 3) * 49152 + nb * 3072 + (i % 3) * 1024, (u32x4){MARK, MARK, MARK, MARK});
    const u16* wsrc = (hw >> 1) ? Wu : Wg;
    unsigned wvo[3];
#pragma unroll
    for (int i = 0; i < 3; ++i) {
      const int un = i * 64 + lane, blk = un / 48, ph = un - 48 * blk;
      const int lg = (ph + 48 - (blk & 1)) % 48, rw = lg / 12, cu = lg - 12 * rw;
      wvo[i] = ((16 * (hw & 1) + 4 * blk + rw) * F + 96 * nb + 8 * cu) * 2;
    }
    auto Wdma = [&](int ks) {
      const int slot = ks & 7;
      const unsigned dst = sb + (slot < 7 ? L_WA + slot * WSLOT : L_WA7) + (hw >> 1) * WUP + (hw & 1) * 3072;
      glds16_s3(wsrc + (size_t)ks * 32 * F, wvo[0], wvo[1] - 1024, wvo[2] - 2048, dst);
    };
#pragma unroll
    for (int s = 0; s < 7; ++s) Wdma(s);
    asm volatile("s_waitcnt vmcnt(21)" ::: "memory");
    {
      unsigned old = 0;
      if (lane == 0) old = __hip_atomic_fetch_add(cnt + 2, 1u, __ATOMIC_RELAXED, __HIP_MEMORY_SCOPE_WORKGROUP);
      if (__builtin_amdgcn_readfirstlane(old) == 3u && lane == 0)
        __hip_atomic_fetch_add((__attribute__((address_space(1))) unsigned*)(ws + WS_CNT + (8 + tb) * 256), 1u, __ATOMIC_RELAXED, __HIP_MEMORY_SCOPE_AGENT);
    }
    u32x4 wp[WREG + 1][3];
#pragma unroll
    for (int s = 0; s <= WREG; ++s)
#pragma unroll
      for (int i = 0; i < 3; ++i) wp[s][i] = *(const u32x4*)((const char*)(wsrc + (size_t)(7 + s) * 32 * F) + wvo[i]);
    SCHED();
    asm volatile("s_waitcnt vmcnt(%0)" :: "n"(3 * (WREG + 1)) : "memory");
    auto Wput = [&](int ks) {
      const int slot = ks & 7;
      const unsigned dst = sb + (slot < 7 ? L_WA + slot * WSLOT : L_WA7) + (hw >> 1) * WUP + (hw & 1) * 3072 + lane * 16;
#pragma unroll
      for (int i = 0; i < 3; ++i) lds_w128(dst + i * 1024, wp[ks - 7][i]);
    };
    STAMP(1, 4);
    RAW_BAR();
    Wput(7);
    RAW_BAR();
    const unsigned goff = g * 4096 + 256 * tb + 16 * t16;
    u32x4 hv[4][4];
    auto Hld = [&](int c, u32x4 (&d)[4]) {
#pragma unroll
      for (int i = 0; i < 4; ++i) d[i] = ld_dev(rs, goff, WS_HT + (64 * c + 16 * hw + 4 * i) * 4096);
      SCHED();
    };
    const unsigned auxo = (lane & 1) ? (8 + tb) * 256 : xcd * 256, auxw = (lane & 1) ? 16 : 256 - NCONV;
    unsigned aux = __builtin_amdgcn_raw_buffer_load_b32(rs, auxo, WS_CNT, 16);
    SCHED();
    Hld(0, hv[0]); Hld(1, hv[1]);
#pragma unroll
    for (int s = 8; s < 8 + WREG; ++s) Wput(s);
    LGKM0_BAR();
    SETTLE(max(max16(hv[0]), max16(hv[1])) == MARK, { Hld(0, hv[0]); Hld(1, hv[1]); });
    Hld(2, hv[2]); Hld(3, hv[3]);
    __builtin_amdgcn_s_setprio(P2PRIO);
    STAMP(2, 0);
    const unsigned lo0 = sb + (16 * hw + g) * 256 + ((t16 ^ (2 * g)) * 16);
    const unsigned lo1 = sb + (16 * hw + 4 + g) * 256 + ((t16 ^ (2 * (4 + g))) * 16);
    const unsigned sqo = sb + (16 * hw + (lane >> 2)) * 256 + ((nb ^ (2 * ((lane >> 2) & 7))) * 16) + p * 4;
    float sq0 = 0.f, sq1 = 0.f;
    unsigned sqv = 0;
    u32x4 dn = {0, 0, 0, 0};
    static_for<0, 10>([&](auto CC) {
      constexpr int c = CC.v;
      u32x4(&hc)[4] = hv[c & 3];
      constexpr int lo[11] = WDSCHED;
      constexpr int t0 = lo[c] < NWH ? lo[c] : NWH, t1 = lo[c + 1] < NWH ? lo[c + 1] : NWH;
      constexpr int p0 = c ? (lo[c ? c - 1 : 0] < NWH ? lo[c ? c - 1 : 0] : NWH) : 0;
      TR(5 * c);
      if (c >= 6) {
        constexpr int younger = 5 * (t0 - p0) + (c - 1 + 4 < 10 ? 4 : 0);
        asm volatile("s_waitcnt vmcnt(%0)" :: "n"(younger) : "memory");
      }
      if (c >= 2) SETTLE(max16(hc) == MARK, Hld(c, hc));
      TR(5 * c + 1);
      if (c >= 3) {
        if (__builtin_amdgcn_readfirstlane(min(min(dn[0], dn[1]), min(dn[2], dn[3]))) < (unsigned)(c - 2)) {
          int sp = 0;
#pragma clang loop unroll(disable)
          do {
            __builtin_amdgcn_s_sleep(1);
            dn = *(volatile LDS_AS const u32x4*)(uintptr_t)(sb + L_DONE);
          } while (__builtin_amdgcn_readfirstlane(min(min(dn[0], dn[1]), min(dn[2], dn[3]))) < (unsigned)(c - 2) && ++sp < (1 << 22));
        }
        CBAR();
        SCHED();
      }
      auto wdld = [&](int n) {
        const int t = t0 + n / 5, jj = n % 5;
        if (t < t1 && !(DIAG & 32)) {
          if (t < NH) wh[t < NH ? NH - 1 - t : 0][jj] = ld_dev(rs, lane * 16, wdoff + ((11 - t) * 5 + jj) * 1024);
          else wx[t < NH ? 0 : t - NH][jj] = ld_dev(rs, lane * 16, wdoff + ((11 - t) * 5 + jj) * 1024);
          SCHED();
        }
      };
      TR(5 * c + 2);
#pragma unroll
      for (int i = 0; i < 4; ++i) if (!(DIAG & 2)) lds_w128(((i & 1) ? lo1 : lo0) + (c % 3) * 16384 + (i >> 1) * 2048, hc[i]);
      CBAR();
      if (lane == 0) __hip_atomic_fetch_add(cnt + 4 + c, 1u, __ATOMIC_RELAXED, __HIP_MEMORY_SCOPE_WORKGROUP);
      CBAR();
      SCHED();
      TR(5 * c + 3);
      if (!(DIAG & 4)) {
      if (c >= 5 && c <= 8) { Wdma(2 * c + 2); Wdma(2 * c + 3); }
      }
      {
        const float lo = bf_lo(sqv), hi = bf_hi(sqv);
        sq0 = fmaf(lo, lo, sq0);
        sq1 = fmaf(hi, hi, sq1);
        asm volatile("" : "+v"(sq0), "+v"(sq1));
        sqv = *(LDS_AS const unsigned*)(uintptr_t)(sqo + (c % 3) * 16384);
      }
      {
        const int pi = 4 * c + hw - 5 * db;
        if (pi >= 0 && pi < 5 && (t16 >> 3) == half) {
#pragma unroll
          for (int i = 0; i < 4; ++i) {
            const int row = 4 * i + g;
            lds_w128(sb + L_RES + (16 * pi + row) * 128 + (((t16 & 7) ^ (((row >> 1) & 3) << 1)) * 16), hc[i]);
          }
        }
      }
      SCHED();
      if (c + 4 < 10 && !(DIAG & 8)) Hld(c + 4, hc);
      if (c == 0) {
        SETTLE(aux < auxw, aux = __builtin_amdgcn_raw_buffer_load_b32(rs, auxo, WS_CNT, 16));
        CBAR();
      }
#pragma unroll
      for (int n = 0; n < 10; ++n) wdld(n);
      if (c >= 2 && c < 9) { dn = *(volatile LDS_AS const u32x4*)(uintptr_t)(sb + L_DONE); SCHED(); }
    });
    sq0 = fmaf(bf_lo(sqv), bf_lo(sqv), sq0);
    sq1 = fmaf(bf_hi(sqv), bf_hi(sqv), sq1);
    {
      auto r = __builtin_amdgcn_permlane32_swap(u_of(sq0), u_of(sq1), false, false);
      float t = f_of(r[0]) + f_of(r[1]);
      auto r2 = __builtin_amdgcn_permlane16_swap(u_of(t), u_of(t), false, false);
      t = f_of(r2[0]) + f_of(r2[1]);
      t += dpp_f<0x124>(t);
      t += dpp_f<0x128>(t);
      *(LDS_AS float*)(uintptr_t)(sb + L_SQ + hw * 32 + (2 * p + (lane >> 5)) * 4) = t;
      asm volatile("s_waitcnt lgkmcnt(0)" ::: "memory");
      unsigned old = 0;
      if (lane == 0) old = __hip_atomic_fetch_add(cnt + 1, 1u, __ATOMIC_RELAXED, __HIP_MEMORY_SCOPE_WORKGROUP);
      if (__builtin_amdgcn_readfirstlane(old) == 3u) {
        CBAR();
        const unsigned a = sb + L_SQ + (lane & 7) * 4;
        const float v = (*(LDS_AS const float*)(uintptr_t)a + *(LDS_AS const float*)(uintptr_t)(a + 32)) +
                        (*(LDS_AS const float*)(uintptr_t)(a + 64) + *(LDS_AS const float*)(uintptr_t)(a + 96));
        __builtin_amdgcn_raw_buffer_store_b32(unmark(u_of(v)), rs, lane < 8 ? lane * 4 : 0x80000000u, WS_SQ + (128 * tb + 8 * nb) * 4, 16);
      }
    }
    __builtin_amdgcn_s_setprio(0);
    STAMP(2, 3);
    LGKM0_BAR();
#pragma unroll
    for (int st = 0; st < 12 - NWH; ++st)
#pragma unroll
      for (int jj = 0; jj < 5; ++jj)
        glds16_asm(ws + wdoff + (st * 5 + jj) * 1024 + lane * 16, sb + (st < 4 ? L_WA + st * 20480 : (st - 4) * 20480) + hw * 5120 + jj * 1024);
#pragma unroll
    for (int t = NWH - 1; t >= NH; --t) {
      const int st = 11 - t;
#pragma unroll
      for (int jj = 0; jj < 5; ++jj) lds_w128(sb + (st < 4 ? L_WA + st * 20480 : (st - 4) * 20480) + hw * 5120 + jj * 1024 + lane * 16, wx[t - NH][jj]);
    }
    asm volatile("s_waitcnt vmcnt(0) lgkmcnt(0)" ::: "memory");
    STAMP(2, 5);
  }
  RAW_BAR();

  {
    int lz = threadIdx.x;
    asm volatile("" : "+v"(lz));
    const int lane = lz & 63, g = lane >> 4, q = (lane >> 2) & 3, p = lane & 3, t16 = lane & 15;
    const unsigned hoff = WS_HH + ((tb * 8 + half * 4) * 192 + 48 * kg) * 256;
    auto loadS = [&](int s, u32x4 (&d)[4]) {
#pragma unroll
      for (int i = 0; i < 4; ++i) d[i] = ld_dev(rs, lane * 16, hoff + i * 49152 + s * 1024);
      SCHED();
    };
    f32x4 a3[4][5];
    if (isC) {
      constexpr int NS = 12 - NH;
      unsigned sv = __builtin_amdgcn_raw_buffer_load_b32(rs, t16 * 4, WS_SQ + (128 * tb + 64 * half + 16 * w) * 4, 16);
      SCHED();
      u32x4 hf[NS][4];
#pragma unroll
      for (int s = 0; s < NS; ++s) loadS(s, hf[s]);
      const unsigned wlb = sb + kg * 5120 + lane * 16;
      auto wreg = [&](int s) -> unsigned { return s < 4 ? L_WA + s * 20480 : (s - 4) * 20480; };
      u32x4 wc[2][5];
#pragma unroll
      for (int jj = 0; jj < 5; ++jj) wc[0][jj] = lds_r128(wlb + wreg(0) + jj * 1024);
      SETTLE(sv == MARK, sv = __builtin_amdgcn_raw_buffer_load_b32(rs, t16 * 4, WS_SQ + (128 * tb + 64 * half + 16 * w) * 4, 16));
      float rr2;
      {
        const float mean = *(LDS_AS const float*)(uintptr_t)(sb + L_ST + (64 * half + 16 * w + t16) * 4);
        float var = fmaf(-mean, mean, f_of(sv) * (1.0f / D));
        var = rnd_bf16(fmaxf(var, 0.f));
        const float sd = rnd_bf16(sqrtf(rnd_bf16(var + EPS)));
        rr2 = 1.0f / (sd * sd);
        asm volatile("" : "+v"(rr2));
      }
      SCHED();
      static_for<0, NS>([&](auto SS) {
        constexpr int s = SS.v;
        SETTLE(max16(hf[s]) == MARK, loadS(s, hf[s]));
        SCHED();
        if (s == 0) STAMP(3, 0);
        if (s == 4) { *(volatile LDS_AS unsigned*)(uintptr_t)(sb + L_DONE + w * 4) = 100u; SCHED(); }
#pragma unroll
        for (int jj = 0; jj < 5; ++jj) {
#pragma unroll
          for (int i = 0; i < 4; ++i) a3[i][jj] = mfma16(as_bf(wc[s & 1][jj]), as_bf(hf[s][(i + 0) & 3]), s == 0 ? (f32x4){0, 0, 0, 0} : a3[i][jj]);
          SCHED();
          if (s + 1 < NS) { wc[(s + 1) & 1][jj] = lds_r128(wlb + wreg(s + 1) + jj * 1024); SCHED(); }
        }
      });
      STAMP(3, 1);
      RAW_BAR();
#pragma unroll
      for (int i = 0; i < 4; ++i)
#pragma unroll
        for (int jj = 0; jj < 5; ++jj) a3[i][jj] += lds_r128f(sb + L_P3 + (kg * 20 + i * 5 + jj) * 1024 + lane * 16);
      static_for<0, 4>([&](auto II) {
        constexpr int i = II.v;
        if (i != w) {
          const int slot = (w - i - 1) & 3;
#pragma unroll
          for (int jj = 0; jj < 5; ++jj) lds_w128f(sb + (i * 15 + slot * 5 + jj) * 1024 + lane * 16, a3[i][jj]);
        }
      });
      f32x4 own[5];
#pragma unroll
      for (int jj = 0; jj < 5; ++jj) own[jj] = w == 0 ? a3[0][jj] : w == 1 ? a3[1][jj] : w == 2 ? a3[2][jj] : a3[3][jj];
      LGKM0_BAR();
      STAMP(3, 2);
#pragma unroll
      for (int jj = 0; jj < 5; ++jj) {
        const unsigned a = sb + (w * 15 + jj) * 1024 + lane * 16;
        const f32x4 o = ((own[jj] + lds_r128f(a)) + (lds_r128f(a + 5120) + lds_r128f(a + 10240))) * rr2;
        const int row = 16 * jj + 4 * g + q;
        const s16x4 hr = tr_read(sb + L_RES + row * 128 + ((w ^ ((row >> 1) & 3)) * 32) + p * 8);
        const u32x2 hu = __builtin_bit_cast(u32x2, hr);
        const unsigned o01 = pk_bf16(o[0], o[1]), o23 = pk_bf16(o[2], o[3]);
        u32x2 ov = {pk_bf16(bf_lo(hu[0]) + bf_lo(o01), bf_hi(hu[0]) + bf_hi(o01)),
                    pk_bf16(bf_lo(hu[1]) + bf_lo(o23), bf_hi(hu[1]) + bf_hi(o23))};
        *(u32x2*)(out + (size_t)(128 * tb + 64 * half + 16 * w + t16) * D + 80 * db + 16 * jj + 4 * g) = ov;
      }
    } else {
      constexpr int HW = 3;
      u32x4 hf[HW][4];
#pragma unroll
      for (int s = 0; s < HW; ++s) loadS(12 - NH + s, hf[s]);
      static_for<0, NH>([&](auto SS) {
        constexpr int s = SS.v;
        SETTLE(max16(hf[s % HW]) == MARK, loadS(12 - NH + s, hf[s % HW]));
        SCHED();
        if (s == 0) STAMP(3, 0);
#pragma unroll
        for (int jj = 0; jj < 5; ++jj)
#pragma unroll
          for (int i = 0; i < 4; ++i) a3[i][jj] = mfma16(as_bf(wh[s][jj]), as_bf(hf[s % HW][i]), s == 0 ? (f32x4){0, 0, 0, 0} : a3[i][jj]);
        SCHED();
        if (s + HW < NH) loadS(12 - NH + s + HW, hf[s % HW]);
      });
      STAMP(3, 1);
      {
        int sp = 0;
#pragma clang loop unroll(disable)
        while (++sp < (1 << 22)) {
          const u32x4 f = *(volatile LDS_AS const u32x4*)(uintptr_t)(sb + L_DONE);
          if (__builtin_amdgcn_readfirstlane(min(min(f[0], f[1]), min(f[2], f[3]))) >= 100u) break;
          __builtin_amdgcn_s_sleep(1);
        }
        CBAR();
      }
#pragma unroll
      for (int i = 0; i < 4; ++i)
#pragma unroll
        for (int jj = 0; jj < 5; ++jj) lds_w128f(sb + L_P3 + (kg * 20 + i * 5 + jj) * 1024 + lane * 16, a3[i][jj]);
      LGKM0_BAR();
      RAW_BAR();
      STAMP(3, 2);
    }
    STAMP(3, 3);
  }
  asm volatile("kf_code_end:");
}

extern "C" void kernel_launch(void* const* d_in, const int* in_sizes, int n_in, void* d_out, int out_size,
                              void* d_ws, size_t ws_size, hipStream_t stream) {
  const u16* q = (const u16*)d_in[0];
  const u16* k = (const u16*)d_in[1];
  const u16* v = (const u16*)d_in[2];
  const u16* shk = (const u16*)d_in[3];
  const u16* ssmk = (const u16*)d_in[4];
  const u16* ssmD = (const u16*)d_in[5];
  const u16* Wg = (const u16*)d_in[7];
  const u16* Wu = (const u16*)d_in[8];
  const u16* Wd = (const u16*)d_in[9];
  char* ws = (char*)d_ws + ((ws_size - WS_END) & ~(size_t)0xFFFFF);
  k0_prep<<<K0GRID, 256, 0, stream>>>(q, k, v, shk, ssmk, ws);
  kf_main<3><<<256, 512, 0, stream>>>(ssmk, ssmD, Wg, Wu, Wd, ws, (u16*)d_out);
}
```

```cpp
#include <hip/hip_runtime.h>
#include <stdint.h>

typedef unsigned short u16;
typedef short s16x4 __attribute__((ext_vector_type(4)));
typedef short s16x8 __attribute__((ext_vector_type(8)));
typedef __bf16 bf16x8 __attribute__((ext_vector_type(8)));
typedef __bf16 bf16x2 __attribute__((ext_vector_type(2)));
typedef float f32x2 __attribute__((ext_vector_type(2)));
typedef float f32x4 __attribute__((ext_vector_type(4)));
typedef unsigned u32x2 __attribute__((ext_vector_type(2)));
typedef unsigned u32x4 __attribute__((ext_vector_type(4)));
#define LDS_AS __attribute__((address_space(3)))
#define DEVI __device__ __forceinline__

constexpr int S = 2048, D = 640, F = 1536;
constexpr float EPS = 1e-5f;
constexpr unsigned MARK = 0xffffffffu;
constexpr int SPIN_CAP = 1 << 14;

#define PITCH 4352
constexpr unsigned RP = PITCH;
constexpr unsigned WS_UT = 0;
constexpr unsigned WS_XT = WS_UT + D * RP;
constexpr unsigned WS_HT = WS_XT + D * RP;
constexpr unsigned WS_ST = WS_HT + D * RP;
constexpr unsigned WS_EP = WS_ST + S * 8;
constexpr unsigned WS_FU = WS_EP + 256;
constexpr unsigned WS_FW = WS_FU + 8 * 5120;
constexpr unsigned WS_FH = WS_FW + 8 * 256;
constexpr unsigned WS_FM = WS_FH + 16 * 256;
constexpr unsigned WS_WD = WS_FM + 8 * 1024;
constexpr unsigned WS_SQ = WS_WD + D * F * 2;
constexpr unsigned WS_HH = WS_SQ + S * 4;
constexpr unsigned WS_END = WS_HH + S * F * 2;

DEVI float bf_lo(unsigned u) { return __builtin_bit_cast(float, u << 16); }
DEVI float bf_hi(unsigned u) { return __builtin_bit_cast(float, u & 0xffff0000u); }
DEVI unsigned pk_bf16(float a, float b) {
  f32x2 v = {a, b};
  bf16x2 r = __builtin_convertvector(v, bf16x2);
  return __builtin_bit_cast(unsigned, r);
}
DEVI float rnd_bf16(float a) { return bf_lo(pk_bf16(a, a)); }
DEVI unsigned unmark(unsigned u) { return u < 0xfffffffeu ? u : 0xfffffffeu; }
DEVI s16x4 tr_read(unsigned lds_byte) {
  return __builtin_amdgcn_ds_read_tr16_b64_v4i16((LDS_AS s16x4*)(uintptr_t)lds_byte);
}
DEVI s16x4 lds_r64(unsigned a) { return *(volatile LDS_AS const s16x4*)(uintptr_t)a; }
DEVI u32x4 lds_r128(unsigned a) { return *(LDS_AS const u32x4*)(uintptr_t)a; }
DEVI void lds_w128(unsigned a, u32x4 v) { *(LDS_AS u32x4*)(uintptr_t)a = v; }
DEVI f32x4 lds_r128f(unsigned a) { return *(LDS_AS const f32x4*)(uintptr_t)a; }
DEVI void lds_w128f(unsigned a, f32x4 v) { *(LDS_AS f32x4*)(uintptr_t)a = v; }
DEVI bf16x8 cat8(s16x4 a, s16x4 b) {
  s16x8 ab = __builtin_shufflevector(a, b, 0, 1, 2, 3, 4, 5, 6, 7);
  return __builtin_bit_cast(bf16x8, ab);
}
DEVI bf16x8 as_bf(u32x4 v) { return __builtin_bit_cast(bf16x8, v); }
DEVI f32x4 mfma16(bf16x8 a, bf16x8 b, f32x4 c) { return __builtin_amdgcn_mfma_f32_16x16x32_bf16(a, b, c, 0, 0, 0); }
DEVI void mfma16_acc(f32x4& acc, bf16x8 a, bf16x8 b) { asm volatile("v_mfma_f32_16x16x32_bf16 %0, %1, %2, %0" : "+v"(acc) : "v"(a), "v"(b)); }
DEVI unsigned lds_addr(const void* p) { return (unsigned)(uintptr_t)(LDS_AS const char*)p; }
DEVI void glds16_asm(const void* gsrc, unsigned lds_dst) {
  unsigned keep;
  asm volatile("s_mov_b32 %0, m0\n\ts_mov_b32 m0, %2\n\ts_nop 0\n\tglobal_load_lds_dwordx4 %1, off sc1\n\ts_mov_b32 m0, %0"
               : "=&s"(keep) : "v"(gsrc), "s"(lds_dst) : "memory");
}
DEVI void glds16_s(const void* sbase, unsigned voff, unsigned lds_dst) {
  unsigned keep;
  asm volatile("s_mov_b32 %0, m0\n\ts_mov_b32 m0, %3\n\ts_nop 0\n\tglobal_load_lds_dwordx4 %1, %2\n\ts_mov_b32 m0, %0"
               : "=&s"(keep) : "v"(voff), "s"(sbase), "s"(lds_dst) : "memory");
}
DEVI void glds16_s3(const void* sbase, unsigned v0, unsigned v1, unsigned v2, unsigned lds_dst) {
  unsigned keep;
  asm volatile("s_mov_b32 %0, m0\n\ts_mov_b32 m0, %5\n\ts_nop 0\n\tglobal_load_lds_dwordx4 %1, %4\n\t"
               "global_load_lds_dwordx4 %2, %4 offset:1024\n\tglobal_load_lds_dwordx4 %3, %4 offset:2048\n\ts_mov_b32 m0, %0"
               : "=&s"(keep) : "v"(v0), "v"(v1), "v"(v2), "s"(sbase), "s"(lds_dst) : "memory");
}
typedef __amdgpu_buffer_rsrc_t rsrc_t;
DEVI u32x4 ld_dev(rsrc_t rs, unsigned voff, unsigned soff) { return __builtin_amdgcn_raw_buffer_load_b128(rs, voff, soff, 16); }
DEVI u32x4 ld_pl(rsrc_t rs, unsigned voff, unsigned soff) { return __builtin_amdgcn_raw_buffer_load_b128(rs, voff, soff, 0); }
DEVI void st_dev(rsrc_t rs, unsigned voff, unsigned soff, u32x4 v) {
  __builtin_amdgcn_raw_buffer_store_b128(v, rs, voff, soff, 16);
  asm volatile("s_nop 1" :: "v"(v) : "memory");
}
DEVI unsigned max4(u32x4 v) { return max(max(v[0], v[1]), max(v[2], v[3])); }
DEVI unsigned max16(const u32x4 (&v)[4]) { return max(max(max4(v[0]), max4(v[1])), max(max4(v[2]), max4(v[3]))); }
template <int CTRL> DEVI float dpp_f(float x) {
  return __builtin_bit_cast(float, __builtin_amdgcn_mov_dpp(__builtin_bit_cast(int, x), CTRL, 0xf, 0xf, true));
}
template <int I> struct IC { static constexpr int v = I; };
template <int I, int N, class Fn> DEVI void static_for(Fn&& f) {
  if constexpr (I < N) { f(IC<I>{}); static_for<I + 1, N>(f); }
}
DEVI float f_of(unsigned u) { return __builtin_bit_cast(float, u); }
DEVI unsigned u_of(float f) { return __builtin_bit_cast(unsigned, f); }

#define STAMP(k, i) do {} while (0)
#define TR(i) do {} while (0)
#define CBAR() asm volatile("" ::: "memory")
#define SCHED() __builtin_amdgcn_sched_barrier(0)
#define RAW_BAR() do { CBAR(); __builtin_amdgcn_s_barrier(); CBAR(); SCHED(); } while (0)
#define LGKM0_BAR() do { asm volatile("s_waitcnt lgkmcnt(0)" ::: "memory"); __builtin_amdgcn_s_barrier(); CBAR(); SCHED(); } while (0)
#define SETTLE(BAD, RELOAD) do { if (__builtin_expect(__any(BAD), 0)) { int sp_ = 0; \
    _Pragma("clang loop unroll(disable)") do { __builtin_amdgcn_s_sleep(8); RELOAD; } while (__any(BAD) && ++sp_ < SPIN_CAP); } } while (0)

#define STATS_MODE 1
constexpr int NCONV = 214;
constexpr int CH_U = 0, CH_K = 4608, CH_Y = CH_K + 4128, CH_C = CH_Y + 8192, CH_BYTES = CH_C + 8192;
constexpr int WSLOT = 12320, WUP = 6176;
constexpr int L_WA7 = 49152, L_RES = 61504, L_ST = L_RES + 10240, L_CS = L_ST + 1024, L_SQ = L_CS + 768, L_CNT = 76000, L_RDY = L_CNT + 16, L_DONE = L_RDY + 48, L_WA = 76800, L_P3 = L_WA;
constexpr int L_SU = CH_Y, L_SX = CH_BYTES + CH_Y;
#define DIAG 0
#define WDSCHED {0, 1, 3, 5, 7, 7, 7, 8, 9, 10, 10}
#define P2PRIO 1
#define NH 5
#define WMODE 0
#define PS0 4
#define PS1 2
#define PS2 2
#define EARLYMK 1
#define PRE 4
#define NWH 10
#define WREGN 5
#define PD 2
constexpr int WREG = WREGN;

DEVI float addf(float a, float b) { float r = a + b; asm("" : "+v"(r)); return r; }
DEVI void conv_store(unsigned base, int ct, int a16, int g, const f32x4 (&z)[4]) {
  f32x4 y = z[0], c = z[3];
  y[1] = addf(y[1], z[1][0]);
  y[2] = addf(addf(y[2], z[1][1]), z[2][0]);
  y[3] = addf(addf(y[3], z[1][2]), addf(z[2][1], z[3][0]));
  c[1] = addf(addf(c[1], z[1][3]), z[2][2]);
  c[2] = addf(c[2], z[2][3]);
  const unsigned o = base + ct * 1024 + g * 256 + ((a16 ^ ((g >> 1) << 3)) * 16);
  lds_w128f(o + CH_Y, y);
  lds_w128f(o + CH_C, c);
}
template <int TOP, int NT, int EX, int NCH>
DEVI void conv_run(unsigned base0, int lane) {
  const int a16 = lane & 15, g = lane >> 4, q = (lane >> 2) & 3, pp = lane & 3;
  const unsigned ab = base0 + CH_U + 128 + 2 * (4 * a16 - 4 - 8 * g) - 64;
  const unsigned aa = base0 + CH_K + 32 + 2 * (32 * g - 4 * q + 4 * pp);
  constexpr int LOW = TOP - NT + 1, NS = 2 * TOP + 2, NA = 2 * TOP + 2;
  f32x4 acc[NT][4], acc0[4];
  bf16x8 A[2][NA], B[3][4];
  auto loadA = [&](int ch, int n) { A[ch & 1][n] = cat8(tr_read(aa + ch * CH_BYTES + 256 * n + 32), tr_read(aa + ch * CH_BYTES + 256 * n)); };
  auto loadStep = [&](int gs) {
    const int ch = gs / NS, st = gs % NS;
    if (st < 2) {
#pragma unroll
      for (int t = NT - 1; t >= 0; --t) loadA(ch, 2 * (LOW + t) + 1 - st);
    }
#pragma unroll
    for (int r = 0; r < 4; ++r) B[gs % 3][r] = cat8(lds_r64(ab + ch * CH_BYTES + 64 * st + r * 1152), lds_r64(ab + ch * CH_BYTES + 64 * st + r * 1152 + 8));
    if (st >= 2 && 2 * LOW + 1 - st > 2 * EX + 1) loadA(ch, 2 * LOW + 1 - st);
    if (2 * EX + 1 - st >= 0) loadA(ch, 2 * EX + 1 - st);
  };
  loadStep(0);
  loadStep(1);
  SCHED();
#pragma unroll
  for (int gs = 0; gs < NCH * NS; ++gs) {
    const int ch = gs / NS, st = gs % NS;
    if (gs + 2 < NCH * NS) loadStep(gs + 2);
#pragma unroll
    for (int t = NT - 1; t >= 0; --t) {
      const int n = 2 * (LOW + t) + 1 - st;
      if (n >= 0) {
#pragma unroll
        for (int r = 0; r < 4; ++r) acc[t][r] = mfma16(A[ch & 1][n], B[gs % 3][r], st == 0 ? (f32x4){0, 0, 0, 0} : acc[t][r]);
      }
    }
    if (2 * EX + 1 - st >= 0) {
#pragma unroll
      for (int r = 0; r < 4; ++r) acc0[r] = mfma16(A[ch & 1][2 * EX + 1 - st], B[gs % 3][r], st == 0 ? (f32x4){0, 0, 0, 0} : acc0[r]);
    }
    if (st == 2 * EX + 2) conv_store(base0 + ch * CH_BYTES, EX, a16, g, acc0);
    if (st >= 2 * LOW + 2 && !(st & 1)) conv_store(base0 + ch * CH_BYTES, (st - 2) / 2, a16, g, acc[(st - 2) / 2 - LOW]);
#pragma unroll
    for (int i = 0; i < 20; ++i) {
      __builtin_amdgcn_sched_group_barrier(0x8, 1, 0);
      __builtin_amdgcn_sched_group_barrier(0x100, 1, 0);
      __builtin_amdgcn_sched_group_barrier(0x2, 1, 0);
    }
    SCHED();
    if (st == NS - 1) { conv_store(base0 + ch * CH_BYTES, TOP, a16, g, acc[NT - 1]); SCHED(); }
  }
}

DEVI void lds_wait_ge(LDS_AS unsigned* word, unsigned want) {
  int sp = 0;
#pragma clang loop unroll(disable)
  while (__builtin_amdgcn_readfirstlane(*(volatile LDS_AS unsigned*)word) < want && ++sp < (1 << 22)) __builtin_amdgcn_s_sleep(1);
  CBAR();
}
DEVI void lds_add(LDS_AS unsigned* word, unsigned v, int lane) {
  CBAR();
  if (lane == 0) __hip_atomic_fetch_add(word, v, __ATOMIC_RELAXED, __HIP_MEMORY_SCOPE_WORKGROUP);
  CBAR();
}

__global__ __launch_bounds__(512, 2) void kf_main(const u16* __restrict__ x, const u16* __restrict__ kin,
                                                  const u16* __restrict__ vin, const u16* __restrict__ shk,
                                                  const u16* __restrict__ ssmk, const u16* __restrict__ ssmD,
                                                  const u16* __restrict__ Wg, const u16* __restrict__ Wu,
                                                  const u16* __restrict__ Wd, char* __restrict__ ws,
                                                  u16* __restrict__ out) {
  __shared__ __attribute__((aligned(16))) char smem[163840];
  const int tid = threadIdx.x, lane = tid & 63;
  const int w = __builtin_amdgcn_readfirstlane(tid >> 6);
  const int b = blockIdx.x, xcd = b & 7, rr = b >> 3;
  const unsigned sb = lds_addr(smem);
  const rsrc_t rs = __builtin_amdgcn_make_buffer_rsrc(ws, 0, WS_END, 0x00020000);
  const int g = lane >> 4, q = (lane >> 2) & 3, p = lane & 3, t16 = lane & 15;
  const int tb = 4 * (xcd & 3) + (rr & 3), nb = 8 * (xcd >> 2) + (rr >> 2);
  const int half = nb & 1, db = nb >> 1;
  const bool isC = w < 4;
  const int hw = w & 3;
  const bool conv = b < NCONV;
  const int nch = conv ? min(3, D - 3 * b) : 0;
  LDS_AS unsigned* cnt = (LDS_AS unsigned*)(uintptr_t)(sb + L_CNT);
  STAMP(0, 0);

  const int tt = 4 * xcd + (rr >> 3), cg = rr & 7;
  unsigned tagv;
  const u32x4 mk = {MARK, MARK, MARK, MARK};
  unsigned wvo[3];
#pragma unroll
  for (int i = 0; i < 3; ++i) {
    const int un = i * 64 + lane, blk = un / 48, ph = un - 48 * blk;
    const int lg = (ph + 48 - (blk & 1)) % 48, rw = lg / 12, cu = lg - 12 * rw;
    wvo[i] = ((4 * blk + rw) * F + 96 * nb + 8 * cu) * 2;
  }
  auto Wdma = [&](int ks, int hq) {
    const int slot = ks & 7;
    const unsigned dst = sb + (slot < 7 ? L_WA + slot * WSLOT : L_WA7) + (hq >> 1) * WUP + (hq & 1) * 3072;
    glds16_s3(((hq >> 1) ? Wu : Wg) + (size_t)(ks * 32 + 16 * (hq & 1)) * F, wvo[0], wvo[1] - 1024, wvo[2] - 2048, dst);
  };
  if (w == 7 && lane < 32) cnt[lane] = 0;
  if (w < 5) {
    const int r2 = tid / 10, cc = tid - 10 * r2;
    const int t0 = 64 * tt + 2 * r2, c0 = 80 * cg + 8 * cc;
    u32x4 kr[5], vr[2], xr[2], sw[4];
#pragma unroll
    for (int i = 0; i < 5; ++i) {
      const int t = t0 - 3 + i;
      kr[i] = *(const u32x4*)(kin + (size_t)(t < 0 ? 0 : t) * D + c0);
      if (t < 0) kr[i] = (u32x4){0, 0, 0, 0};
    }
#pragma unroll
    for (int i = 0; i < 2; ++i) {
      vr[i] = *(const u32x4*)(vin + (size_t)(t0 + i) * D + c0);
      xr[i] = *(const u32x4*)(x + (size_t)(t0 + i) * D + c0);
    }
#pragma unroll
    for (int i = 0; i < 4; ++i) sw[i] = *(const u32x4*)(shk + (size_t)c0 * 4 + 8 * i);
    tagv = __builtin_amdgcn_raw_buffer_load_b32(rs, 0, WS_EP, 16);
    RAW_BAR();
#pragma unroll
    for (int i = 0; i < 2; ++i) {
      u32x4 o;
#pragma unroll
      for (int cp = 0; cp < 4; ++cp) {
        float r[2];
#pragma unroll
        for (int hh = 0; hh < 2; ++hh) {
          const unsigned w01 = sw[cp][2 * hh], w23 = sw[cp][2 * hh + 1];
          float kk[4];
#pragma unroll
          for (int j = 0; j < 4; ++j) kk[j] = hh ? bf_hi(kr[i + 3 - j][cp]) : bf_lo(kr[i + 3 - j][cp]);
          float a = kk[3] * bf_hi(w23);
          a = fmaf(kk[2], bf_lo(w23), a);
          a = fmaf(kk[1], bf_hi(w01), a);
          a = fmaf(kk[0], bf_lo(w01), a);
          r[hh] = a;
        }
        const unsigned ks = pk_bf16(r[0], r[1]);
        o[cp] = pk_bf16(bf_lo(ks) * bf_lo(vr[i][cp]), bf_hi(ks) * bf_hi(vr[i][cp]));
      }
      lds_w128(sb + L_SU + (2 * r2 + i) * 176 + cc * 16, o);
      lds_w128(sb + L_SX + (2 * r2 + i) * 176 + cc * 16, xr[i]);
    }
    STAMP(0, 1);
    lds_add(cnt + 30, 1u, lane);
    lds_wait_ge(cnt + 30, 5u);
    STAMP(0, 2);
  } else if (w < 7) {
    const int row = 64 * tt + 8 * cg + 4 * (w - 5) + g;
    u32x4 xr[5];
#pragma unroll
    for (int i = 0; i < 5; ++i) xr[i] = *(const u32x4*)(x + (size_t)row * D + (t16 + 16 * i) * 8);
    tagv = __builtin_amdgcn_raw_buffer_load_b32(rs, 0, WS_EP, 16);
#pragma unroll
    for (int i = 0; i < 4; ++i) st_dev(rs, w - 5 < nch ? lane * 16 : 0x80000000u, WS_HT + min(3 * b + w - 5, D - 1) * RP + i * 1024, mk);
    RAW_BAR();

    float s = 0.f;
#pragma unroll
    for (int i = 0; i < 5; ++i)
#pragma unroll
      for (int j = 0; j < 4; ++j) s += bf_lo(xr[i][j]) + bf_hi(xr[i][j]);
    s += dpp_f<0xB1>(s); s += dpp_f<0x4E>(s); s += dpp_f<0x124>(s); s += dpp_f<0x128>(s);
    const float mean = s * (1.0f / D);
    float qq = 0.f;
#pragma unroll
    for (int i = 0; i < 5; ++i)
#pragma unroll
      for (int j = 0; j < 4; ++j) {
        const float a = bf_lo(xr[i][j]) - mean, c = bf_hi(xr[i][j]) - mean;
        qq = fmaf(a, a, qq);
        qq = fmaf(c, c, qq);
      }
    qq += dpp_f<0xB1>(qq); qq += dpp_f<0x4E>(qq); qq += dpp_f<0x124>(qq); qq += dpp_f<0x128>(qq);
    const float var = rnd_bf16(qq * (1.0f / D));
    const unsigned gran = pk_bf16(mean, sqrtf(rnd_bf16(var + EPS)));
    __builtin_amdgcn_raw_buffer_store_b64((u32x2){gran, __builtin_amdgcn_readfirstlane(tagv) + 1u}, rs, t16 == 0 ? g * 8 : 0x80000000u, WS_ST + (row - g) * 8, 16);
    STAMP(0, 1);
  } else {
    u32x4 kq[12];
#pragma unroll
    for (int i = 0; i < 12; ++i) kq[i] = *(const u32x4*)(ssmk + (size_t)min(3 * b + i / 4, D - 1) * S + (i & 3) * 512 + lane * 8);
    tagv = __builtin_amdgcn_raw_buffer_load_b32(rs, 0, WS_EP, 16);
    unsigned pf0, pf1;
    {
      unsigned len;
      asm volatile("kf_code_begin:\n\ts_mov_b32 %0, kf_code_end - kf_code_begin" : "=s"(len));
      const char* pc = (const char*)__builtin_amdgcn_s_getpc();
      const unsigned o = (rr * 2) * 1024 + lane * 16;
      pf0 = *(const __attribute__((address_space(1))) unsigned*)(pc + (o + 64 < len ? o : 0));
      pf1 = *(const __attribute__((address_space(1))) unsigned*)(pc + (o + 1088 < len ? o + 1024 : 0));
    }
#pragma unroll
    for (int i = 0; i < 4; ++i) st_dev(rs, 2 < nch ? lane * 16 : 0x80000000u, WS_HT + min(3 * b + 2, D - 1) * RP + i * 1024, mk);
    st_dev(rs, lane < 2 ? lane * 16 : 0x80000000u, WS_SQ + (128 * tb + 8 * nb) * 4, mk);
    LGKM0_BAR();

#pragma unroll
    for (int lc = 0; lc < 3; ++lc) {
      const unsigned cb = sb + lc * CH_BYTES;
      lds_w128(lane < 32 ? cb + CH_U + (lane >> 3) * 1152 + (lane & 7) * 16 : cb + CH_K + (lane & 1) * 16, (u32x4){0, 0, 0, 0});
    }
    if (conv) {
#pragma unroll
      for (int i = 0; i < 12; ++i) lds_w128(sb + (i / 4) * CH_BYTES + CH_K + 32 + (i & 3) * 1024 + lane * 16, kq[i]);
    }
    lds_add(cnt + 3, 64u, lane);
    asm volatile("" :: "v"(pf0), "v"(pf1));
    STAMP(0, 1);
  }
  const unsigned tag = __builtin_amdgcn_readfirstlane(tagv) + 1u;
  if (w < 5) {
    const int c = 80 * cg + 16 * w + t16;
    const unsigned colb = (16 * w + 4 * p) * 2;
    u32x4 tu[2], tx[2];
#pragma unroll
    for (int si = 0; si < 2; ++si) {
      const int ci = 4 * si + g;
      const unsigned a = sb + L_SU + (4 * (8 * (ci & 1) + q) + (ci >> 1)) * 176 + colb, a2 = sb + L_SX + (8 * ci + q) * 176 + colb;
      tu[si] = __builtin_bit_cast(u32x4, cat8(tr_read(a), tr_read(a + 16 * 176)));
      tx[si] = __builtin_bit_cast(u32x4, cat8(tr_read(a2), tr_read(a2 + 4 * 176)));
    }
    SCHED();
#pragma unroll
    for (int si = 0; si < 2; ++si) st_dev(rs, c * RP + tt * 128 + (4 * si + g) * 16, WS_UT, tu[si]);
#pragma unroll
    for (int si = 0; si < 2; ++si) st_dev(rs, c * RP + tt * 128 + (4 * si + g) * 16, WS_XT, tx[si]);
    asm volatile("s_waitcnt vmcnt(0)" ::: "memory");
    __builtin_amdgcn_raw_buffer_store_b32(tag, rs, lane < 8 ? lane * 5120 : 0x80000000u, WS_FU + ((5 * cg + w) * 32 + tt) * 4, 16);
    lds_add(cnt + 3, 1u, lane);
    STAMP(0, 3);
  }

  f32x4 acc[3][8];
  const int kg = w & 3;
  const unsigned wdoff = WS_WD + (db * 48 + 12 * kg) * 5 * 1024;
  u32x4 wh[NH][5];
  u32x4 wx[5][5];

  if (isC) {
    u32x4 xv[3], st4[4];
    float dval[3];
    auto ldst = [&]() {
#pragma unroll
      for (int i = 0; i < 4; ++i) st4[i] = ld_dev(rs, tid * 64 + i * 16, WS_ST);
    };
    auto stale = [&]() -> bool {
      bool bad = false;
#pragma unroll
      for (int i = 0; i < 4; ++i) bad |= st4[i][1] != tag || st4[i][3] != tag;
      return bad;
    };
    if (conv) {
#pragma unroll
      for (int i = 0; i < 3; ++i) dval[i] = bf_lo(ssmD[min(3 * b + i, D - 1)]);
      if (w == 3) {
        const unsigned fo = (((lane >> 5) ? min(3 * b + 2, D - 1) : 3 * b) >> 4) * 128 + (lane & 31) * 4;
        int sp = 0;
#pragma clang loop unroll(disable)
        while (__any(__builtin_amdgcn_raw_buffer_load_b32(rs, fo, WS_FU + xcd * 5120, 16) != tag) && ++sp < SPIN_CAP) __builtin_amdgcn_s_sleep(PS0);
        lds_add(cnt + 27, 1u, lane);
      } else {
        lds_wait_ge(cnt + 27, 1u);
      }
      STAMP(1, 0);
      u32x4 uq[4];
      if (w < 3) {
#pragma unroll
        for (int i = 0; i < 4; ++i) uq[i] = ld_dev(rs, lane * 16, WS_UT + min(3 * b + w, D - 1) * RP + i * 1024);
      }
#pragma unroll
      for (int i = 0; i < 3; ++i) xv[i] = ld_dev(rs, tid * 16, WS_XT + min(3 * b + i, D - 1) * RP);
      ldst();
      SCHED();
      lds_add(cnt + 29, 1u, lane);
      if (w < 3) {
        const unsigned uo = sb + w * CH_BYTES + CH_U + ((lane >> 1) & 3) * 1152 + 128 + ((lane >> 3) * 2 + (lane & 1)) * 16;
#pragma unroll
        for (int i = 0; i < 4; ++i) lds_w128(uo + i * 256, uq[i]);
        lds_add(cnt + 3, 16u, lane);
      }
      lds_wait_ge(cnt + 3, 117u);
      STAMP(1, 1);
      if (w < 3) {
        conv_run<7, 4, 1, 1>(sb + w * CH_BYTES, lane);
      } else {
        conv_run<3, 2, 0, 3>(sb, lane);
      }
      STAMP(1, 2);
      SETTLE(stale(), { ldst(); STAMP(1, 7); });
      STAMP(1, 5);
      float mu[8], rsd[8];
#pragma unroll
      for (int e = 0; e < 8; ++e) {
        mu[e] = bf_lo(st4[e >> 1][2 * (e & 1)]);
        rsd[e] = __builtin_amdgcn_rcpf(bf_hi(st4[e >> 1][2 * (e & 1)]));
      }
      lds_add(cnt, 1u, lane);
      lds_wait_ge(cnt, 4u);
      STAMP(1, 6);
      lds_wait_ge(cnt + 2, 4u);
      STAMP(1, 3);
      {
        auto pos = [&](int blk) -> unsigned { const int gg = blk & 3; return (blk >> 6) * 1024 + gg * 256 + ((((blk >> 2) & 15) ^ ((gg >> 1) << 3)) * 16); };
        const unsigned p0 = sb + pos(2 * tid), p1 = sb + pos(2 * tid + 1), pm = sb + pos(tid ? 2 * tid - 1 : 0);
        f32x4 y0[3], y1[3], c0[3], c1[3];
        unsigned uu[3][4];
#pragma unroll
        for (int lc = 0; lc < 3; ++lc) {
          y0[lc] = lds_r128f(p0 + lc * CH_BYTES + CH_Y);
          c0[lc] = lds_r128f(pm + lc * CH_BYTES + CH_C);
          y1[lc] = lds_r128f(p1 + lc * CH_BYTES + CH_Y);
          c1[lc] = lds_r128f(p0 + lc * CH_BYTES + CH_C);
#pragma unroll
          for (int r = 0; r < 4; ++r) uu[lc][r] = *(LDS_AS const unsigned*)(uintptr_t)(sb + lc * CH_BYTES + CH_U + r * 1152 + 128 + tid * 4);
        }
        SCHED();
#pragma unroll
        for (int lc = 0; lc < 3; ++lc) {
          float h[8];
#pragma unroll
          for (int e = 0; e < 8; ++e) {
            float y = e < 4 ? y0[lc][e & 3] : y1[lc][e & 3];
            if ((e & 3) < 3) y += e < 4 ? (tid ? c0[lc][(e & 3) + 1] : 0.f) : c1[lc][(e & 3) + 1];
            const float u = e < 4 ? bf_lo(uu[lc][e & 3]) : bf_hi(uu[lc][e & 3]);
            const float xx = (e & 1) ? bf_hi(xv[lc][e >> 1]) : bf_lo(xv[lc][e >> 1]);
            h[e] = fmaf((xx - mu[e]) * rsd[e], fmaf(u, dval[lc], y), xx);
          }
          u32x4 o = {unmark(pk_bf16(h[0], h[1])), unmark(pk_bf16(h[2], h[3])), unmark(pk_bf16(h[4], h[5])), unmark(pk_bf16(h[6], h[7]))};
          st_dev(rs, lc < nch ? tid * 16 : 0x80000000u, WS_HT + min(3 * b + lc, D - 1) * RP, o);
          SCHED();
        }
      }
    } else {
      constexpr int RS = 272;
      const unsigned sm = sb + (w == 0 ? 0 : w == 1 ? 20224 : w == 2 ? 45312 : 54016);
      for (int u = (b - NCONV) * 6 + w; u < min(240, (b - NCONV) * 6 + 6); u += 4) {
        const int ks2 = u / 5, dq = u % 5;
        u32x4 v8[8];
#pragma unroll
        for (int i = 0; i < 8; ++i) v8[i] = *(const u32x4*)(Wd + (size_t)(32 * ks2 + 4 * i + g) * D + 128 * dq + 8 * t16);
#pragma unroll
        for (int i = 0; i < 8; ++i) lds_w128(sm + (4 * i + g) * RS + t16 * 16, v8[i]);
        CBAR();
#pragma unroll
        for (int t = 0; t < 8; ++t) {
          const int dt = 8 * dq + t;
          const unsigned a = sm + (8 * g + q) * RS + t * 32 + p * 8;
          st_dev(rs, lane * 16, WS_WD + (((dt / 5) * 48 + ks2) * 5 + (dt % 5)) * 1024, __builtin_bit_cast(u32x4, cat8(tr_read(a), tr_read(a + 4 * RS))));
        }
        CBAR();
      }
      asm volatile("s_waitcnt vmcnt(0)" ::: "memory");
      lds_add(cnt, 1u, lane);
      if (w == 0) {
        lds_wait_ge(cnt, 4u);
        __builtin_amdgcn_raw_buffer_store_b32(tag, rs, lane < 8 ? lane * 256 : 0x80000000u, WS_FW + (b - NCONV) * 4, 16);
      }
    }
    STAMP(1, 4);
    LGKM0_BAR();
    unsigned wa[2][3];
    {
      const int r = 4 * g + q;
#pragma unroll
      for (int jj = 0; jj < 3; ++jj) {
        wa[0][jj] = sb + L_WA7 + (p & 1) * WUP + (r >> 2) * 768 + ((12 * (r & 3) + 3 * w + jj + ((r >> 2) & 1)) % 48) * 16 + (p >> 1) * 8;
        wa[1][jj] = wa[0][jj] + (L_WA + 3 * WSLOT - L_WA7);
        asm volatile("" : "+v"(wa[0][jj]), "+v"(wa[1][jj]));
      }
    }
    auto wadr = [&](int slot, int jj) -> unsigned {
      return slot == 7 ? wa[0][jj] : slot < 3 ? wa[0][jj] + (L_WA - L_WA7 + slot * WSLOT) : wa[1][jj] + (slot - 3) * WSLOT;
    };
    auto loadW = [&](int slot, bf16x8 (&dst)[3]) {
#pragma unroll
      for (int jj = 0; jj < 3; ++jj) dst[jj] = cat8(tr_read(wadr(slot, jj)), tr_read(wadr(slot, jj) + 3072));
    };
    bf16x8 wr[WREG][3], wl[3];
#pragma unroll
    for (int s = 0; s < WREG; ++s) loadW(s, wr[s]);
    LGKM0_BAR();
    u32x4 ones_u = {0x3f803f80u, 0x3f803f80u, 0x3f803f80u, 0x3f803f80u};
    asm volatile("" : "+v"(ones_u));
    const bf16x8 ones = as_bf(ones_u);
    f32x4 cs[3] = {{0, 0, 0, 0}, {0, 0, 0, 0}, {0, 0, 0, 0}};
#pragma unroll
    for (int s = 0; s < WREG; ++s)
#pragma unroll
      for (int jj = 0; jj < 3; ++jj) cs[jj] = mfma16(wr[s][jj], ones, cs[jj]);
    RAW_BAR();
#pragma unroll
    for (int s = WREG; s < 8 + WREG; ++s) {
      loadW(s & 7, wl);
#pragma unroll
      for (int jj = 0; jj < 3; ++jj) cs[jj] = mfma16(wl[jj], ones, cs[jj]);
    }
#pragma unroll
    for (int jj = 0; jj < 3; ++jj) {
      lds_w128f(sb + L_CS + w * 192 + jj * 64 + g * 16, cs[jj]);
      cs[jj] = (f32x4){0, 0, 0, 0};
    }
    SCHED();
    unsigned ta[8];
    {
      const unsigned lb = sb + (4 * g + q) * 256 + 32 * (4 * (g & 1) + q) + 16 * (p >> 1) + 8 * (p & 1);
#pragma unroll
      for (int i = 0; i < 8; ++i) ta[i] = lb ^ (32 * ((i + 2 * w) & 7));
    }
#pragma unroll
    for (int jj = 0; jj < 3; ++jj)
#pragma unroll
      for (int i = 0; i < 8; ++i) acc[jj][i] = (f32x4){0, 0, 0, 0};
    s16x4 af[PD + 1][4][2], wq[2][3][2];
    auto hoff = [&](int hs) -> unsigned { const int ks = hs >> 1; return ((ks >> 1) % 3) * 16384 + (ks & 1) * 8192; };
    f32x4 sacc[2] = {{0, 0, 0, 0}, {0, 0, 0, 0}};
    auto waitReady = [&](unsigned f, int c) {
      if (__builtin_expect(__builtin_amdgcn_readfirstlane(f) < 4u, 0)) {
        int sp = 0;
        TR(2 * c);
#pragma clang loop unroll(disable)
        do {
          __builtin_amdgcn_s_sleep(1);
          f = *(volatile LDS_AS const unsigned*)(uintptr_t)(sb + L_RDY + 4 * c);
        } while (__builtin_amdgcn_readfirstlane(f) < 4u && ++sp < (1 << 22));
        TR(2 * c + 1);
      }
      CBAR();
      SCHED();
    };
#pragma unroll
    for (int jj = 0; jj < 3; ++jj)
#pragma unroll
      for (int i = 0; i < 8; ++i) asm volatile("" : "+v"(acc[jj][i]));
    asm volatile("" : "+v"(sacc[0]), "+v"(sacc[1]));
    unsigned rdy = 0;
    waitReady(rdy, 0);
    STAMP(2, 1);
#pragma unroll
    for (int i = 0; i < 4 * PD; ++i) { af[i >> 2][i & 3][0] = tr_read(ta[i]); af[i >> 2][i & 3][1] = tr_read(ta[i] + 4096); }
    SCHED();
    static_for<0, 40>([&](auto HS) {
      constexpr int hs = HS.v, ks = hs >> 1, cur = hs & 1, cb = hs % (PD + 1), hn = hs + PD, nx = hn % (PD + 1), nh = hn & 1;
      if (hs + 2 < 40 && ((hs + 2) & 3) == 0) waitReady(rdy, (hs + 2) / 4);
      if ((hs & 3) == 1 && hs + 3 < 40) { rdy = *(volatile LDS_AS const unsigned*)(uintptr_t)(sb + L_RDY + 4 * ((hs + 3) / 4)); SCHED(); }
      if ((hs & 3) == 0 && hs >= 4 && hs <= 28) {
        *(volatile LDS_AS unsigned*)(uintptr_t)(sb + L_DONE + w * 4) = hs / 4;
        SCHED();
      }
      constexpr bool refill = !cur && ks + 1 >= WREG && ks + 1 < 20;
      constexpr int wo = (ks + 1) & 7;
      int gap = 0;
      auto filler = [&]() {
        const int n = gap++;
        if (n < 8) {
          if (hn < 40) af[nx][n >> 1][n & 1] = tr_read(ta[4 * nh + (n >> 1)] + hoff(hn) + (n & 1) * 4096);
        } else if (refill && n < 14) {
          wq[(ks + 1) & 1][(n - 8) >> 1][n & 1] = tr_read(wadr(wo, (n - 8) >> 1) + (n & 1) * 3072);
        }
        SCHED();
      };
#pragma unroll
      for (int jj = 0; jj < 3; ++jj) {
        const bf16x8 wv = ks < WREG ? wr[ks < WREG ? ks : 0][jj] : cat8(wq[ks & 1][jj][0], wq[ks & 1][jj][1]);
#pragma unroll
        for (int i = 0; i < 4; ++i) {
          mfma16_acc(acc[jj][4 * cur + i], wv, cat8(af[cb][i][0], af[cb][i][1]));
          SCHED();
          filler();
        }
        if (ks >= 8 + WREG && cur) { cs[jj] = mfma16(wv, ones, cs[jj]); SCHED(); }
        if (!cur && jj < 2) { mfma16_acc(sacc[jj], ones, cat8(af[cb][jj][0], af[cb][jj][1])); SCHED(); filler(); }
      }
    });
    asm volatile("s_nop 15" ::: "memory");
    SCHED();
#pragma unroll
    for (int i = 0; i < 2; ++i) *(LDS_AS float*)(uintptr_t)(sb + L_ST + ((2 * w + i) * 16 + t16) * 4) = sacc[i][0] * (1.0f / D);
    STAMP(2, 2);
    LGKM0_BAR();
    STAMP(2, 4);
    int lz = threadIdx.x;
    asm volatile("" : "+v"(lz));
    const int lane = lz & 63, g = lane >> 4, t16 = lane & 15;
#pragma unroll
    for (int jj = 0; jj < 3; ++jj) cs[jj] += lds_r128f(sb + L_CS + w * 192 + jj * 64 + g * 16);
    float st[8];
#pragma unroll
    for (int i = 0; i < 8; ++i) st[i] = rnd_bf16(*(LDS_AS const float*)(uintptr_t)(sb + L_ST + (((i + 2 * w) & 7) * 16 + t16) * 4));
    SCHED();
#pragma unroll
    for (int hf = 0; hf < 2; ++hf) {
      const unsigned vo = (tb * 8 + ((4 * hf + 2 * (g >> 1) + (g & 1) + 2 * w) & 7)) * 49152 + t16 * 16;
#pragma unroll
      for (int jj = 0; jj < 3; ++jj) {
        unsigned X[2][2];
#pragma unroll
        for (int pr = 0; pr < 2; ++pr) {
          const int i = 4 * hf + 2 * pr;
          const f32x4 a = acc[jj][i] - st[i] * cs[jj], c = acc[jj][i + 1] - st[i + 1] * cs[jj];
          float hv4[4];
#pragma unroll
          for (int e = 0; e < 4; ++e) {
            const auto sw = __builtin_amdgcn_permlane16_swap(u_of(a[e]), u_of(c[e]), false, false);
            hv4[e] = __builtin_elementwise_maximum(f_of(sw[0]), 0.f) * f_of(sw[1]);
          }
          X[pr][0] = unmark(pk_bf16(hv4[0], hv4[1]));
          X[pr][1] = unmark(pk_bf16(hv4[2], hv4[3]));
        }
        const auto s0 = __builtin_amdgcn_permlane32_swap(X[0][0], X[1][0], false, false);
        const auto s1 = __builtin_amdgcn_permlane32_swap(X[0][1], X[1][1], false, false);
        st_dev(rs, vo, WS_HH + (nb * 12 + w * 3 + jj) * 256, (u32x4){s0[0], s1[0], s0[1], s1[1]});
        SCHED();
      }
    }
    STAMP(2, 5);
    asm volatile("s_waitcnt vmcnt(0)" ::: "memory");
  } else {
    if (w == 5) {
      if (conv) lds_wait_ge(cnt + 29, 4u); else __builtin_amdgcn_s_sleep(100);
      const unsigned fo = (5 * (lane >> 3) + 4) * 128 + (lane & 7) * 16;
      int sp = 0;
#pragma clang loop unroll(disable)
      for (; sp < SPIN_CAP; ++sp) {
        const u32x4 f = ld_dev(rs, fo, WS_FU + ((xcd + 4) & 7) * 5120);
        if (!__any(f[0] != tag || f[1] != tag || f[2] != tag || f[3] != tag)) break;
        __builtin_amdgcn_s_sleep(8);
      }
      lds_add(cnt + 29, 1u, lane);
    }
    lds_wait_ge(cnt + 29, conv ? 5u : 1u);
    STAMP(1, 1);
    asm volatile("s_waitcnt vmcnt(0)" ::: "memory");
    {
      unsigned old = 0;
      if (lane == 0) old = __hip_atomic_fetch_add(cnt + 2, 1u, __ATOMIC_RELAXED, __HIP_MEMORY_SCOPE_WORKGROUP);
      if (__builtin_amdgcn_readfirstlane(old) == 3u) __builtin_amdgcn_raw_buffer_store_b32(tag, rs, lane < 8 ? lane * 1024 : 0x80000000u, WS_FM + b * 4, 16);
    }
#pragma unroll
    for (int s = 0; s < 7; ++s) Wdma(s, hw);
    const char* wsrc = (const char*)(((hw >> 1) ? Wu : Wg) + (size_t)16 * (hw & 1) * F);
    u32x4 wp[WREG + 1][3];
#pragma unroll
    for (int s = 0; s <= WREG; ++s)
#pragma unroll
      for (int i = 0; i < 3; ++i) wp[s][i] = *(const u32x4*)(wsrc + (size_t)(7 + s) * 32 * F * 2 + wvo[i]);
    SCHED();
    asm volatile("s_waitcnt vmcnt(%0)" :: "n"(3 * (WREG + 1)) : "memory");
    auto Wput = [&](int ks) {
      const int slot = ks & 7;
      const unsigned dst = sb + (slot < 7 ? L_WA + slot * WSLOT : L_WA7) + (hw >> 1) * WUP + (hw & 1) * 3072 + lane * 16;
#pragma unroll
      for (int i = 0; i < 3; ++i) lds_w128(dst + i * 1024, wp[ks - 7][i]);
    };
    {
      u32x4 fm = ld_dev(rs, lane * 16, WS_FM + xcd * 1024);
      SETTLE(fm[0] != tag || fm[1] != tag || fm[2] != tag || fm[3] != tag, fm = ld_dev(rs, lane * 16, WS_FM + xcd * 1024));
      CBAR();
    }
    STAMP(1, 4);
    RAW_BAR();
    Wput(7);
    RAW_BAR();
    const unsigned goff = g * RP + 256 * tb + 16 * t16;
    u32x4 hv[4][4];
    auto Hld = [&](int c, u32x4 (&d)[4]) {
#pragma unroll
      for (int i = 0; i < 4; ++i) d[i] = ld_dev(rs, goff, WS_HT + (64 * c + 16 * hw + 4 * i) * RP);
      SCHED();
    };
    const unsigned auxo = min(lane, 255 - NCONV) * 4;
    unsigned aux = __builtin_amdgcn_raw_buffer_load_b32(rs, auxo, WS_FW + xcd * 256, 16);
    SCHED();
    Hld(0, hv[0]); Hld(1, hv[1]);
#pragma unroll
    for (int s = 8; s < 8 + WREG; ++s) Wput(s);
    LGKM0_BAR();
    SETTLE(max(max16(hv[0]), max16(hv[1])) == MARK, { Hld(0, hv[0]); Hld(1, hv[1]); });
    Hld(2, hv[2]); Hld(3, hv[3]);
    __builtin_amdgcn_s_setprio(P2PRIO);
    STAMP(2, 0);
    const unsigned lo0 = sb + (16 * hw + g) * 256 + ((t16 ^ (2 * g)) * 16);
    const unsigned lo1 = sb + (16 * hw + 4 + g) * 256 + ((t16 ^ (2 * (4 + g))) * 16);
    const unsigned sqo = sb + (16 * hw + (lane >> 2)) * 256 + ((nb ^ (2 * ((lane >> 2) & 7))) * 16) + p * 4;
    float sq0 = 0.f, sq1 = 0.f;
    unsigned sqv = 0;
    u32x4 dn = {0, 0, 0, 0};
    static_for<0, 10>([&](auto CC) {
      constexpr int c = CC.v;
      u32x4(&hc)[4] = hv[c & 3];
      constexpr int lo[11] = WDSCHED;
      constexpr int t0 = lo[c] < NWH ? lo[c] : NWH, t1 = lo[c + 1] < NWH ? lo[c + 1] : NWH;
      constexpr int p0 = c ? (lo[c ? c - 1 : 0] < NWH ? lo[c ? c - 1 : 0] : NWH) : 0;

      if (c >= 6) {
        constexpr int younger = 5 * (t0 - p0) + (c - 1 + 4 < 10 ? 4 : 0);
        asm volatile("s_waitcnt vmcnt(%0)" :: "n"(younger) : "memory");
      }
      if (c >= 2) SETTLE(max16(hc) == MARK, Hld(c, hc));

      if (c >= 3) {
        if (__builtin_amdgcn_readfirstlane(min(min(dn[0], dn[1]), min(dn[2], dn[3]))) < (unsigned)(c - 2)) {
          int sp = 0;
#pragma clang loop unroll(disable)
          do {
            __builtin_amdgcn_s_sleep(1);
            dn = *(volatile LDS_AS const u32x4*)(uintptr_t)(sb + L_DONE);
          } while (__builtin_amdgcn_readfirstlane(min(min(dn[0], dn[1]), min(dn[2], dn[3]))) < (unsigned)(c - 2) && ++sp < (1 << 22));
        }
        CBAR();
        SCHED();
      }
      auto wdld = [&](int n) {
        const int t = t0 + n / 5, jj = n % 5;
        if (t < t1 && !(DIAG & 32)) {
          if (t < NH) wh[t < NH ? NH - 1 - t : 0][jj] = ld_dev(rs, lane * 16, wdoff + ((11 - t) * 5 + jj) * 1024);
          else wx[t < NH ? 0 : t - NH][jj] = ld_dev(rs, lane * 16, wdoff + ((11 - t) * 5 + jj) * 1024);
          SCHED();
        }
      };

#pragma unroll
      for (int i = 0; i < 4; ++i) if (!(DIAG & 2)) lds_w128(((i & 1) ? lo1 : lo0) + (c % 3) * 16384 + (i >> 1) * 2048, hc[i]);
      CBAR();
      if (lane == 0) __hip_atomic_fetch_add(cnt + 4 + c, 1u, __ATOMIC_RELAXED, __HIP_MEMORY_SCOPE_WORKGROUP);
      CBAR();
      SCHED();

      if (!(DIAG & 4)) {
      if (c <= 8 && 2 * c + 2 >= 8 + WREG) Wdma(2 * c + 2, hw);
      if (c <= 8 && 2 * c + 3 >= 8 + WREG) Wdma(2 * c + 3, hw);
      }
      {
        const float lo = bf_lo(sqv), hi = bf_hi(sqv);
        sq0 = fmaf(lo, lo, sq0);
        sq1 = fmaf(hi, hi, sq1);
        asm volatile("" : "+v"(sq0), "+v"(sq1));
        sqv = *(LDS_AS const unsigned*)(uintptr_t)(sqo + (c % 3) * 16384);
      }
      {
        const int pi = 4 * c + hw - 5 * db;
        if (pi >= 0 && pi < 5 && (t16 >> 3) == half) {
#pragma unroll
          for (int i = 0; i < 4; ++i) {
            const int row = 4 * i + g;
            lds_w128(sb + L_RES + (16 * pi + row) * 128 + (((t16 & 7) ^ (((row >> 1) & 3) << 1)) * 16), hc[i]);
          }
        }
      }
      SCHED();
      if (c + 4 < 10 && !(DIAG & 8)) Hld(c + 4, hc);
      if (c == 0) {
#pragma unroll
        for (int i = 0; i < 6; ++i) st_dev(rs, lane * 16, WS_HH + (tb * 8 + 2 * hw + i / 3) * 49152 + nb * 3072 + (i % 3) * 1024, mk);
      }
      if (c == 5) {
        unsigned old = 0;
        if (lane == 0) old = __hip_atomic_fetch_add(cnt + 28, 1u, __ATOMIC_RELAXED, __HIP_MEMORY_SCOPE_WORKGROUP);
        if (__builtin_amdgcn_readfirstlane(old) == 3u) __builtin_amdgcn_raw_buffer_store_b32(tag, rs, lane ? 0x80000000u : 0u, WS_FH + tb * 256 + nb * 4, 16);
      }
      if (c == 9) aux = __builtin_amdgcn_raw_buffer_load_b32(rs, t16 * 4, WS_FH + tb * 256, 16);
      if (c == 0) {
        SETTLE(aux != tag, aux = __builtin_amdgcn_raw_buffer_load_b32(rs, auxo, WS_FW + xcd * 256, 16));
        CBAR();
      }
#pragma unroll
      for (int n = 0; n < 10; ++n) wdld(n);
      if (c >= 2 && c < 9) { dn = *(volatile LDS_AS const u32x4*)(uintptr_t)(sb + L_DONE); SCHED(); }
    });
    sq0 = fmaf(bf_lo(sqv), bf_lo(sqv), sq0);
    sq1 = fmaf(bf_hi(sqv), bf_hi(sqv), sq1);
    {
      auto r = __builtin_amdgcn_permlane32_swap(u_of(sq0), u_of(sq1), false, false);
      float t = f_of(r[0]) + f_of(r[1]);
      auto r2 = __builtin_amdgcn_permlane16_swap(u_of(t), u_of(t), false, false);
      t = f_of(r2[0]) + f_of(r2[1]);
      t += dpp_f<0x124>(t);
      t += dpp_f<0x128>(t);
      *(LDS_AS float*)(uintptr_t)(sb + L_SQ + hw * 32 + (2 * p + (lane >> 5)) * 4) = t;
      asm volatile("s_waitcnt lgkmcnt(0)" ::: "memory");
      unsigned old = 0;
      if (lane == 0) old = __hip_atomic_fetch_add(cnt + 1, 1u, __ATOMIC_RELAXED, __HIP_MEMORY_SCOPE_WORKGROUP);
      if (__builtin_amdgcn_readfirstlane(old) == 3u) {
        CBAR();
        const unsigned a = sb + L_SQ + (lane & 7) * 4;
        const float v = (*(LDS_AS const float*)(uintptr_t)a + *(LDS_AS const float*)(uintptr_t)(a + 32)) +
                        (*(LDS_AS const float*)(uintptr_t)(a + 64) + *(LDS_AS const float*)(uintptr_t)(a + 96));
        __builtin_amdgcn_raw_buffer_store_b32(unmark(u_of(v)), rs, lane < 8 ? lane * 4 : 0x80000000u, WS_SQ + (128 * tb + 8 * nb) * 4, 16);
      }
    }
    __builtin_amdgcn_s_setprio(0);
    STAMP(2, 3);
    LGKM0_BAR();
    SETTLE(aux != tag, aux = __builtin_amdgcn_raw_buffer_load_b32(rs, t16 * 4, WS_FH + tb * 256, 16));
#pragma unroll
    for (int st = 0; st < 12 - NWH; ++st)
#pragma unroll
      for (int jj = 0; jj < 5; ++jj)
        glds16_asm(ws + wdoff + (st * 5 + jj) * 1024 + lane * 16, sb + (st < 4 ? L_WA + st * 20480 : (st - 4) * 20480) + hw * 5120 + jj * 1024);
#pragma unroll
    for (int t = NWH - 1; t >= NH; --t) {
      const int st = 11 - t;
#pragma unroll
      for (int jj = 0; jj < 5; ++jj) lds_w128(sb + (st < 4 ? L_WA + st * 20480 : (st - 4) * 20480) + hw * 5120 + jj * 1024 + lane * 16, wx[t - NH][jj]);
    }
    asm volatile("s_waitcnt vmcnt(0) lgkmcnt(0)" ::: "memory");
    STAMP(2, 5);
  }
  RAW_BAR();

  {
    int lz = threadIdx.x;
    asm volatile("" : "+v"(lz));
    const int lane = lz & 63, g = lane >> 4, q = (lane >> 2) & 3, p = lane & 3, t16 = lane & 15;
    const unsigned hoff = WS_HH + ((tb * 8 + half * 4) * 192 + 48 * kg) * 256;
    auto loadS = [&](int s, u32x4 (&d)[4]) {
#pragma unroll
      for (int i = 0; i < 4; ++i) d[i] = ld_dev(rs, lane * 16, hoff + i * 49152 + s * 1024);
      SCHED();
    };
    f32x4 a3[4][5];
    if (isC) {
      constexpr int NS = 2 * (12 - NH), CW = 8;
      unsigned sv = __builtin_amdgcn_raw_buffer_load_b32(rs, t16 * 4, WS_SQ + (128 * tb + 64 * half + 16 * w) * 4, 16);
      SCHED();
      const unsigned ho = WS_HH + ((tb * 8 + half * 4 + (w ^ 0)) * 192 + 48 * (w & 1)) * 256, ho2 = WS_HH + ((tb * 8 + half * 4 + (w ^ 1)) * 192 + 48 * (w & 1)) * 256;
      u32x4 hf[CW][2];
      auto loadC = [&](int s) {
        hf[s % CW][0] = ld_dev(rs, lane * 16, ho + ((s & 1) * 24 + (s >> 1)) * 1024);
        hf[s % CW][1] = ld_dev(rs, lane * 16, ho2 + ((s & 1) * 24 + (s >> 1)) * 1024);
        SCHED();
      };
#pragma unroll
      for (int s = 0; s < CW; ++s) loadC(s);
      const unsigned wlb = sb + (w & 1) * 5120 + lane * 16;
      unsigned wlb2 = wlb + L_WA, wlb3 = wlb + L_WA + 40960;
      asm volatile("" : "+v"(wlb2), "+v"(wlb3));
      auto wadr = [&](int s, int jj) -> unsigned { const int u = s >> 1; return (u < 2 ? wlb2 + u * 20480 : u < 4 ? wlb3 + (u - 2) * 20480 : wlb + (u - 4) * 20480) + (s & 1) * 10240 + jj * 1024; };
      u32x4 wc[2][5];
#pragma unroll
      for (int jj = 0; jj < 5; ++jj) wc[0][jj] = lds_r128(wadr(0, jj));
      SETTLE(sv == MARK, sv = __builtin_amdgcn_raw_buffer_load_b32(rs, t16 * 4, WS_SQ + (128 * tb + 64 * half + 16 * w) * 4, 16));
      float rr2;
      {
        const float mean = *(LDS_AS const float*)(uintptr_t)(sb + L_ST + (64 * half + 16 * w + t16) * 4);
        float var = fmaf(-mean, mean, f_of(sv) * (1.0f / D));
        var = rnd_bf16(fmaxf(var, 0.f));
        const float sd = rnd_bf16(sqrtf(rnd_bf16(var + EPS)));
        rr2 = 1.0f / (sd * sd);
        asm volatile("" : "+v"(rr2));
      }
      SCHED();
      static_for<0, NS>([&](auto SS) {
        constexpr int s = SS.v;
        SETTLE(max(max4(hf[s % CW][0]), max4(hf[s % CW][1])) == MARK, loadC(s));
        SCHED();
        if (s == 0) STAMP(3, 0);
        if (s == 8) { *(volatile LDS_AS unsigned*)(uintptr_t)(sb + L_DONE + w * 4) = 100u; SCHED(); }
#pragma unroll
        for (int jj = 0; jj < 5; ++jj) {
#pragma unroll
          for (int i = 0; i < 2; ++i) a3[i][jj] = mfma16(as_bf(wc[s & 1][jj]), as_bf(hf[s % CW][i]), s == 0 ? (f32x4){0, 0, 0, 0} : a3[i][jj]);
          SCHED();
          if (s + 1 < NS) { wc[(s + 1) & 1][jj] = lds_r128(wadr(s + 1, jj)); SCHED(); }
        }
        if (s + CW < NS) loadC(s + CW);
      });
      STAMP(3, 1);
      RAW_BAR();
#pragma unroll
      for (int jj = 0; jj < 5; ++jj) lds_w128f(sb + (w ^ 1) * 5120 + jj * 1024 + lane * 16, a3[1][jj]);
      LGKM0_BAR();
      STAMP(3, 2);
#pragma unroll
      for (int jj = 0; jj < 5; ++jj) {
        const unsigned a = sb + (w * 5 + jj) * 1024 + lane * 16;
        const f32x4 o = (((a3[0][jj] + lds_r128f(a)) + (lds_r128f(a + L_P3) + lds_r128f(a + L_P3 + 20480))) + (lds_r128f(a + L_P3 + 40960) + lds_r128f(a + L_P3 + 61440))) * rr2;
        const int row = 16 * jj + 4 * g + q;
        const s16x4 hr = tr_read(sb + L_RES + row * 128 + ((w ^ ((row >> 1) & 3)) * 32) + p * 8);
        const u32x2 hu = __builtin_bit_cast(u32x2, hr);
        const unsigned o01 = pk_bf16(o[0], o[1]), o23 = pk_bf16(o[2], o[3]);
        u32x2 ov = {pk_bf16(bf_lo(hu[0]) + bf_lo(o01), bf_hi(hu[0]) + bf_hi(o01)),
                    pk_bf16(bf_lo(hu[1]) + bf_lo(o23), bf_hi(hu[1]) + bf_hi(o23))};
        *(u32x2*)(out + (size_t)(128 * tb + 64 * half + 16 * w + t16) * D + 80 * db + 16 * jj + 4 * g) = ov;
      }
    } else {
      constexpr int HW = 3;
      u32x4 hf[HW][4];
#pragma unroll
      for (int s = 0; s < HW; ++s) loadS(12 - NH + s, hf[s]);
      static_for<0, NH>([&](auto SS) {
        constexpr int s = SS.v;
        SETTLE(max16(hf[s % HW]) == MARK, loadS(12 - NH + s, hf[s % HW]));
        SCHED();
        if (s == 0) STAMP(3, 0);
#pragma unroll
        for (int jj = 0; jj < 5; ++jj)
#pragma unroll
          for (int i = 0; i < 4; ++i) a3[i][jj] = mfma16(as_bf(wh[s][jj]), as_bf(hf[s % HW][i]), s == 0 ? (f32x4){0, 0, 0, 0} : a3[i][jj]);
        SCHED();
        if (s + HW < NH) loadS(12 - NH + s + HW, hf[s % HW]);
      });
      STAMP(3, 1);
      {
        int sp = 0;
#pragma clang loop unroll(disable)
        while (++sp < (1 << 22)) {
          const u32x4 f = *(volatile LDS_AS const u32x4*)(uintptr_t)(sb + L_DONE);
          if (__builtin_amdgcn_readfirstlane(min(min(f[0], f[1]), min(f[2], f[3]))) >= 100u) break;
          __builtin_amdgcn_s_sleep(1);
        }
        CBAR();
      }
#pragma unroll
      for (int i = 0; i < 4; ++i)
#pragma unroll
        for (int jj = 0; jj < 5; ++jj) lds_w128f(sb + L_P3 + (kg * 20 + i * 5 + jj) * 1024 + lane * 16, a3[i][jj]);
      RAW_BAR();
      LGKM0_BAR();
      STAMP(3, 2);
    }
    STAMP(3, 3);
  }
  if (b == 0 && tid == 0) __builtin_amdgcn_raw_buffer_store_b32(tag, rs, 0, WS_EP, 16);
}

__global__ void kf_marker() { asm volatile("kf_code_end:"); }

extern "C" void kernel_launch(void* const* d_in, const int* in_sizes, int n_in, void* d_out, int out_size,
                              void* d_ws, size_t ws_size, hipStream_t stream) {
  const u16* q = (const u16*)d_in[0];
  const u16* k = (const u16*)d_in[1];
  const u16* v = (const u16*)d_in[2];
  const u16* shk = (const u16*)d_in[3];
  const u16* ssmk = (const u16*)d_in[4];
  const u16* ssmD = (const u16*)d_in[5];
  const u16* Wg = (const u16*)d_in[7];
  const u16* Wu = (const u16*)d_in[8];
  const u16* Wd = (const u16*)d_in[9];
  char* ws = (char*)d_ws;
  kf_main<<<256, 512, 0, stream>>>(q, k, v, shk, ssmk, ssmD, Wg, Wu, Wd, ws, (u16*)d_out);
}
```
